```python
import torch

def _softmax(x, axis=-1):
    return torch.softmax(x, dim=axis)
def _gelu(x):
    return torch.nn.functional.gelu(x, approximate="tanh")
def _silu(x):
    return torch.nn.functional.silu(x)
def _sigmoid(x):
    return torch.sigmoid(x)
def _layernorm(x, axis=-1, eps=1e-5):
    m = x.mean(dim=axis, keepdim=True)
    v = x.var(dim=axis, keepdim=True, unbiased=False)
    return (x - m) / torch.sqrt(v + eps)
def _scatter_add(tgt, idx, src, axis=0):
    return tgt.clone().index_add_(axis, idx, src)
def _segment_sum(src, seg, n):
    out = torch.zeros((n,) + src.shape[1:], dtype=src.dtype)
    return out.index_add_(0, seg, src)
def _pad(x, pw):
    flat = tuple(v for pair in reversed(pw) for v in pair)
    return torch.nn.functional.pad(x, flat)

kernel_name = 'concat_heads__sliding_local_dilated__mha_block__prompt'

def setup_inputs():

    _w = (lambda a: (lambda t: t.to(torch.bfloat16) if t.is_floating_point() else t)(torch.as_tensor(a)))
    def _A_setup():
        B, S, D = 1, 2048, 640
        return {
            "x": _w((lambda *s: torch.randn(*s, dtype=torch.bfloat16) * 0.4)(B, S, D)),
            "wqkv": _w((lambda *s: torch.randn(*s, dtype=torch.bfloat16) * 0.4)(D, 3 * D)),
            "wo": _w((lambda *s: torch.randn(*s, dtype=torch.bfloat16) * 0.05)(D, D)),
        }
    _A_in = _A_setup()

    def _B_setup():
        B, S, D, H = 1, 2048, 640, 32
        return {
            "x": _w((lambda *s: torch.randn(*s, dtype=torch.bfloat16) * 0.2)(B, S, D)),
            "wqkv": _w((lambda *s: torch.randn(*s, dtype=torch.bfloat16) * 0.4)(D, 3 * D)),
            "wo": _w((lambda *s: torch.randn(*s, dtype=torch.bfloat16) * 0.1)(D, D)),
        }
    _B_in = _B_setup()

    return {
        "A_x": _A_in["x"],
        "A_wqkv": _A_in["wqkv"],
        "A_wo": _A_in["wo"],
        "B_x": _B_in["x"],
        "B_wqkv": _B_in["wqkv"],
        "B_wo": _B_in["wo"],
    }

def reference(A_x, A_wqkv, A_wo, B_x, B_wqkv, B_wo):
    def _A_ref(x, wqkv, wo):
        B, S, D = x.shape
        H = 32; Dh = D // H
        W, dil = 8, 2
        qkv = (lambda x, *d: x.permute(*d))((x @ wqkv).reshape(B, S, 3, H, Dh), 2, 0, 3, 1, 4)
        q, k, v = qkv[0], qkv[1], qkv[2]
        scale = Dh ** -0.5
        pos = torch.arange(S)
        off = torch.arange(W) * dil
        idx = pos[:, None] - off[None, :]
        mask = idx >= 0
        idx_c = torch.where(mask, idx, 0).reshape(-1)
        kg = k[:, :, idx_c, :].reshape(B, H, S, W, Dh)
        vg = v[:, :, idx_c, :].reshape(B, H, S, W, Dh)
        logits = torch.einsum("bhsd,bhswd->bhsw", q, kg) * scale
        logits = torch.where(mask[None, None, :, :], logits, -1e9)
        attn = _softmax(logits, axis=-1)
        o = torch.einsum("bhsw,bhswd->bhsd", attn, vg)
        return (lambda x, *d: x.permute(*d))(o, 0, 2, 1, 3).reshape(B, S, D) @ wo

    def _B_ref(x, wqkv, wo):
        B, S, D = x.shape
        H = 32
        Dh = D // H
        qkv = (lambda x, *d: x.permute(*d))((x @ wqkv).reshape(B, S, 3, H, Dh), 2, 0, 3, 1, 4)
        q, k, v = qkv[0], qkv[1], qkv[2]
        scores = torch.einsum("bhqd,bhkd->bhqk", q, k) * (Dh ** -0.5)
        p = _softmax(scores, axis=-1)
        o = torch.einsum("bhqk,bhkd->bhqd", p, v)
        return (lambda x, *d: x.permute(*d))(o, 0, 2, 1, 3).reshape(B, S, D) @ wo

    a = _A_ref(A_x, A_wqkv, A_wo)
    b = _B_ref(B_x, B_wqkv, B_wo)
    return torch.concatenate([a, b], axis=-1)
```

```cpp
#include <hip/hip_runtime.h>
#include <hip/hip_bf16.h>
#include <stdint.h>

typedef __attribute__((ext_vector_type(8))) short bf16x8;
typedef __attribute__((ext_vector_type(4))) short bf16x4;
typedef __attribute__((ext_vector_type(4))) float f32x4;
typedef __attribute__((ext_vector_type(2))) float f32x2;
typedef __attribute__((ext_vector_type(16))) float f32x16;
typedef __attribute__((ext_vector_type(4))) unsigned u32x4;
typedef __attribute__((ext_vector_type(2))) unsigned u32x2;
typedef __attribute__((ext_vector_type(2))) __bf16 bf16x2_t;
typedef __attribute__((address_space(3))) const char* lds_cptr;
typedef short v4i16_t __attribute__((ext_vector_type(4)));

#define DI __device__ __forceinline__
#define LDS_PTR(T, p) ((__attribute__((address_space(3))) T*)(p))

constexpr int S = 2048, D = 640, N3 = 1920, NH = 32, DH = 20;
constexpr float SM_SCALE = 0.22360679774997896f;
constexpr float LOG2E = 1.4426950408889634f;
constexpr float QSCALE = SM_SCALE * LOG2E;

constexpr size_t WS_QB = 0;
constexpr size_t WS_KV = WS_QB + (size_t)NH * 64 * 1536;
constexpr size_t WS_KN = WS_KV + (size_t)NH * 64 * 3072;
constexpr size_t WS_O = WS_KN + 4096;
constexpr size_t WS_AQ = WS_O + (size_t)2 * S * D * 2;
constexpr size_t WS_END = WS_AQ + (size_t)NH * S * 136 + 4096;

DI unsigned pack2(float lo, float hi) {
  f32x2 v = {lo, hi};
  return __builtin_bit_cast(unsigned, __builtin_convertvector(v, bf16x2_t));
}
DI float dot2(unsigned a, unsigned b, float c) {
  return __builtin_amdgcn_fdot2_f32_bf16(__builtin_bit_cast(bf16x2_t, a), __builtin_bit_cast(bf16x2_t, b), c, false);
}
DI float bf_lo(unsigned u) { return __uint_as_float(u << 16); }
DI float bf_hi(unsigned u) { return __uint_as_float(u & 0xffff0000u); }
DI void glds16(const void* g, void* l) {
  __builtin_amdgcn_global_load_lds((const unsigned*)g, (unsigned*)l, 16, 0, 0);
}
DI bf16x4 tr_read(const char* p) {
  return __builtin_bit_cast(bf16x4, __builtin_amdgcn_ds_read_tr16_b64_v4i16(LDS_PTR(v4i16_t, p)));
}
#define WAIT_VM(n) asm volatile("s_waitcnt vmcnt(" #n ")" ::: "memory")
#define RAW_BAR() do { asm volatile("s_waitcnt lgkmcnt(0)" ::: "memory"); __builtin_amdgcn_s_barrier(); asm volatile("" ::: "memory"); } while (0)

constexpr int K1_XB = 128 * 128;
constexpr int K1_WB = 64 * 480;
constexpr int K1_STAGE = K1_XB + K1_WB;
constexpr int K1_LDS = 3 * K1_STAGE + 256;
constexpr int CT_PITCH = 488;
constexpr int VT_OFF = 128 * CT_PITCH;
constexpr int VT_PITCH = 264;

__global__ __launch_bounds__(512, 2) void k1_qkv(const unsigned short* __restrict__ xA, const unsigned short* __restrict__ wA,
                                                 const unsigned short* __restrict__ xB, const unsigned short* __restrict__ wB,
                                                 char* __restrict__ ws) {
  extern __shared__ __attribute__((aligned(16))) char lds[];
  const int tid = threadIdx.x, lane = tid & 63;
  const int w = __builtin_amdgcn_readfirstlane(tid >> 6);
  const int xcd = blockIdx.x & 7, slot = blockIdx.x >> 3;
  const int prob = xcd >> 2, mblk = (xcd & 3) * 4 + (slot >> 3), nblk = slot & 7;
  const char* X = (const char*)(prob ? xB : xA);
  const char* W = (const char*)(prob ? wB : wA);
  const int m0 = mblk * 128, h0 = nblk * 4;

  const char* src[6];
  int dst[6];
#pragma unroll
  for (int j = 0; j < 2; ++j) {
    const int i = w + 8 * j, row = i * 8 + (lane >> 3), c = (lane & 7) ^ (row & 7);
    src[j] = X + (size_t)(m0 + row) * (D * 2) + c * 16;
    dst[j] = i * 1024;
  }
#pragma unroll
  for (int j = 2; j < 6; ++j) {
    int iw = w + 8 * (j - 2);
    if (iw >= 30) iw -= 8;
    const int idx = iw * 64 + lane, rho = idx / 30, cc = idx - rho * 30;
    const int k = (rho & ~12) | ((rho & 4) << 1) | ((rho & 8) >> 1);
    const int seg = cc / 10, c10 = cc - seg * 10;
    src[j] = W + (size_t)k * (N3 * 2) + seg * (D * 2) + h0 * (DH * 2) + c10 * 16;
    dst[j] = K1_XB + iw * 1024;
  }
  auto stage = [&](int kt, int buf) {
    char* b = lds + buf * K1_STAGE;
#pragma unroll
    for (int j = 0; j < 2; ++j) glds16(src[j] + kt * 128, b + dst[j]);
#pragma unroll
    for (int j = 2; j < 6; ++j) glds16(src[j] + (size_t)kt * (64 * N3 * 2), b + dst[j]);
  };

  const int wm = w >> 2, wn = w & 3;
  const int l15 = lane & 15, kq = lane >> 4;
  int xoff[4][2];
#pragma unroll
  for (int mb = 0; mb < 4; ++mb) {
    const int row = wm * 64 + mb * 16 + l15;
#pragma unroll
    for (int ks = 0; ks < 2; ++ks) xoff[mb][ks] = row * 128 + (((ks * 4 + kq) ^ (row & 7)) << 4);
  }
  const int woff = K1_XB + ((kq >> 1) * 16 + (kq & 1) * 4 + (l15 >> 2)) * 480 + (l15 & 3) * 8 + wn * 128;

  f32x4 acc[4][4];
#pragma unroll
  for (int a = 0; a < 4; ++a)
#pragma unroll
    for (int b = 0; b < 4; ++b) acc[a][b] = (f32x4){0.f, 0.f, 0.f, 0.f};

  stage(0, 0);
  stage(1, 1);
#pragma unroll 1
  for (int kt = 0; kt < 10; ++kt) {
    if (kt < 9) WAIT_VM(6); else WAIT_VM(0);
    RAW_BAR();
    if (kt + 2 < 10) stage(kt + 2, (kt + 2) % 3);
    const char* b = lds + (kt % 3) * K1_STAGE;
#pragma unroll
    for (int ks = 0; ks < 2; ++ks) {
      bf16x8 xf[4];
#pragma unroll
      for (int mb = 0; mb < 4; ++mb) xf[mb] = *LDS_PTR(const bf16x8, b + xoff[mb][ks]);
#pragma unroll
      for (int nb = 0; nb < 4; ++nb) {
        const bf16x4 lo = tr_read(b + woff + ks * (32 * 480) + nb * 32);
        const bf16x4 hi = tr_read(b + woff + ks * (32 * 480) + 8 * 480 + nb * 32);
        const bf16x8 wf = __builtin_shufflevector(lo, hi, 0, 1, 2, 3, 4, 5, 6, 7);
#pragma unroll
        for (int mb = 0; mb < 4; ++mb) acc[nb][mb] = __builtin_amdgcn_mfma_f32_16x16x32_bf16(wf, xf[mb], acc[nb][mb], 0, 0, 0);
      }
    }
  }
  RAW_BAR();

#pragma unroll
  for (int nb = 0; nb < 4; ++nb) {
    const int ng = wn * 4 + nb;
    if (ng < 15) {
      const float sc = (prob && ng < 5) ? QSCALE : 1.f;
#pragma unroll
      for (int mb = 0; mb < 4; ++mb) {
        const int m = wm * 64 + mb * 16 + l15, n = ng * 16 + kq * 4;
        const f32x4 v = acc[nb][mb] * sc;
        const unsigned p0 = pack2(v[0], v[1]), p1 = pack2(v[2], v[3]);
        if (prob && ng >= 10) {
          char* t = lds + VT_OFF + (n - 160) * VT_PITCH + m * 2;
          *LDS_PTR(unsigned short, t) = (unsigned short)p0;
          *LDS_PTR(unsigned short, t + VT_PITCH) = (unsigned short)(p0 >> 16);
          *LDS_PTR(unsigned short, t + 2 * VT_PITCH) = (unsigned short)p1;
          *LDS_PTR(unsigned short, t + 3 * VT_PITCH) = (unsigned short)(p1 >> 16);
        } else {
          *LDS_PTR(u32x2, lds + m * CT_PITCH + n * 2) = (u32x2){p0, p1};
        }
      }
    }
  }
  __syncthreads();

  if (prob) {
#pragma unroll
    for (int it = 0; it < 6; ++it) {
      const int c = it * 512 + tid;
      const int seg = c >= 1536;
      const int cc = c - seg * 1536;
      const int r32 = cc & 31, t = cc >> 5;
      const int part = t % 3, t2 = t / 3, blk = t2 & 3, hh = t2 >> 2;
      const char* p = lds + (blk * 32 + r32) * CT_PITCH + seg * 160 + hh * 40 + part * 16;
      const u32x2 a = *LDS_PTR(const u32x2, p);
      u32x2 b2;
      if (part < 2) b2 = *LDS_PTR(const u32x2, p + 8);
      else b2 = (u32x2){seg ? 0x3F80u : 0u, 0u};
      const u32x4 v = {a[0], a[1], b2[0], b2[1]};
      const size_t gb = (size_t)(h0 + hh) * 64 + mblk * 4 + blk;
      char* g = seg ? (ws + WS_KV + gb * 3072) : (ws + WS_QB + gb * 1536);
      *(u32x4*)(g + part * 512 + r32 * 16) = v;
    }
#pragma unroll
    for (int it = 0; it < 3; ++it) {
      const int c = it * 512 + tid;
      const int d = c % 24, t = c / 24;
      const int sh = t & 3, blk = (t >> 2) & 3, hh = t >> 4;
      u32x4 v;
      if (d < 20) {
        const char* p = lds + VT_OFF + (hh * 20 + d) * VT_PITCH + (blk * 32 + (sh >> 1) * 16 + (sh & 1) * 4) * 2;
        const u32x2 a = *LDS_PTR(const u32x2, p), b2 = *LDS_PTR(const u32x2, p + 16);
        v = (u32x4){a[0], a[1], b2[0], b2[1]};
      } else {
        const unsigned o = d == 20 ? 0x3F803F80u : 0u;
        v = (u32x4){o, o, o, o};
      }
      const size_t gb = (size_t)(h0 + hh) * 64 + mblk * 4 + blk;
      *(u32x4*)(ws + WS_KV + gb * 3072 + 1536 + sh * 384 + d * 16) = v;
    }
    {
      const int hh = tid >> 7, row = tid & 127;
      const char* p = lds + row * CT_PITCH + 160 + hh * 40;
      float s = 0.f;
#pragma unroll
      for (int i = 0; i < 5; ++i) {
        const u32x2 a = *LDS_PTR(const u32x2, p + i * 8);
        float x;
        x = bf_lo(a[0]); s = fmaf(x, x, s); x = bf_hi(a[0]); s = fmaf(x, x, s);
        x = bf_lo(a[1]); s = fmaf(x, x, s); x = bf_hi(a[1]); s = fmaf(x, x, s);
      }
#pragma unroll
      for (int o = 32; o; o >>= 1) s = fmaxf(s, __shfl_xor(s, o));
      if (lane == 0) ((float*)(ws + WS_KN))[(h0 + hh) * 32 + mblk * 2 + (w & 1)] = s;
    }
  } else {
#pragma unroll
    for (int it = 0; it < 15; ++it) {
      const int c = it * 512 + tid;
      const int u = c % 15, t = c / 15, row = t & 127, hh = t >> 7;
      const int seg = u / 5, uu = u - seg * 5;
      const u32x2 a = *LDS_PTR(const u32x2, lds + row * CT_PITCH + seg * 160 + hh * 40 + uu * 8);
      *(u32x2*)(ws + WS_AQ + ((size_t)(h0 + hh) * S + m0 + row) * 136 + u * 8) = a;
    }
  }
}

constexpr int K2_STAGE = 2 * 4 * 3072;
constexpr int K2_LOC = 11 * 1024;
constexpr int K2_LDS = 3 * K2_STAGE + 4 * K2_LOC;
#define MFMA32(a, b, c) __builtin_amdgcn_mfma_f32_32x32x16_bf16(a, b, c, 0, 0, 0)

DI void exp_pack(const f32x16& s, bf16x8& p0, bf16x8& p1) {
  u32x4 a, b;
#pragma unroll
  for (int i = 0; i < 4; ++i) {
    a[i] = pack2(__builtin_amdgcn_exp2f(s[2 * i]), __builtin_amdgcn_exp2f(s[2 * i + 1]));
    b[i] = pack2(__builtin_amdgcn_exp2f(s[8 + 2 * i]), __builtin_amdgcn_exp2f(s[8 + 2 * i + 1]));
  }
  p0 = __builtin_bit_cast(bf16x8, a);
  p1 = __builtin_bit_cast(bf16x8, b);
}
DI float swap32(float x) {
  return __shfl_xor(x, 32);
}
DI float sumsq8(bf16x8 f) {
  const u32x4 u = __builtin_bit_cast(u32x4, f);
  float s = 0.f;
#pragma unroll
  for (int i = 0; i < 4; ++i) { float x = bf_lo(u[i]); s = fmaf(x, x, s); x = bf_hi(u[i]); s = fmaf(x, x, s); }
  return s;
}

DI void attn_fallback(const char* kvh, bf16x8 q0, bf16x8 q1, int lane, f32x16& O) {
  const int r32 = lane & 31, h = lane >> 5;
  const int dv = r32 < 24 ? r32 : (r32 < 28 ? r32 - 4 : r32 - 12);
  float m = -INFINITY;
  O = (f32x16){};
#pragma unroll 1
  for (int t = 0; t < 64; ++t) {
    const char* g = kvh + (size_t)t * 3072;
    const bf16x8 k0 = *(const bf16x8*)(g + h * 512 + r32 * 16);
    const bf16x8 k1 = *(const bf16x8*)(g + 1024 + r32 * 16);
    const bf16x8 v0 = *(const bf16x8*)(g + 1536 + h * 384 + dv * 16);
    const bf16x8 v1 = *(const bf16x8*)(g + 1536 + 768 + h * 384 + dv * 16);
    f32x16 s = MFMA32(k0, q0, (f32x16){});
    s = MFMA32(k1, q1, s);
    float pm = s[0];
#pragma unroll
    for (int i = 1; i < 16; ++i) pm = fmaxf(pm, s[i]);
    pm = fmaxf(pm, swap32(pm));
    const float mn = fmaxf(m, pm);
    const float alpha = __builtin_amdgcn_exp2f(m - mn);
    m = mn;
#pragma unroll
    for (int i = 0; i < 16; ++i) { s[i] -= mn; O[i] *= alpha; }
    bf16x8 p0, p1;
    exp_pack(s, p0, p1);
    O = MFMA32(v0, p0, O);
    O = MFMA32(v1, p1, O);
  }
}

__global__ __launch_bounds__(768, 3) void k2_attn(char* __restrict__ ws) {
  extern __shared__ __attribute__((aligned(16))) char lds[];
  const int tid = threadIdx.x, lane = tid & 63;
  const int w = __builtin_amdgcn_readfirstlane(tid >> 6);

  if (w >= 8) {
    const int task = blockIdx.x * 4 + (w - 8), hA = task >> 5, r0 = (task & 31) * 64;
    char* L = lds + 3 * K2_STAGE + (w - 8) * K2_LOC;
    const char* g = ws + WS_AQ + ((ptrdiff_t)hA * S + r0 - 14) * 136 + lane * 16;
#pragma unroll
    for (int i = 0; i < 11; ++i) glds16(g + i * 1024, L + i * 1024);
    __builtin_amdgcn_s_barrier();
    WAIT_VM(0);
    const int s = r0 + lane;
    const char* me = L + (lane + 14) * 136;
    unsigned q[10];
#pragma unroll
    for (int i = 0; i < 5; ++i) { const u32x2 a = *LDS_PTR(const u32x2, me + i * 8); q[2 * i] = a[0]; q[2 * i + 1] = a[1]; }
    float lg[8];
    float mx = -INFINITY;
#pragma unroll
    for (int j = 0; j < 8; ++j) {
      const int back = (s - 2 * j >= 0) ? 2 * j : s;
      const char* kr = me - back * 136 + 40;
      float a0 = 0.f, a1 = 0.f;
#pragma unroll
      for (int i = 0; i < 5; ++i) {
        const u32x2 a = *LDS_PTR(const u32x2, kr + i * 8);
        const unsigned x0 = a[0], x1 = a[1];
        a0 = dot2(x0, q[2 * i], a0);
        a1 = dot2(x1, q[2 * i + 1], a1);
      }
      lg[j] = (s - 2 * j >= 0) ? (a0 + a1) * QSCALE : -INFINITY;
      mx = fmaxf(mx, lg[j]);
    }
    float sum = 0.f;
#pragma unroll
    for (int j = 0; j < 8; ++j) { lg[j] = __builtin_amdgcn_exp2f(lg[j] - mx); sum += lg[j]; }
    const float inv = 1.f / sum;
    float o[20];
#pragma unroll
    for (int i = 0; i < 20; ++i) o[i] = 0.f;
#pragma unroll
    for (int j = 0; j < 8; ++j) {
      const int back = (s - 2 * j >= 0) ? 2 * j : s;
      const char* vr = me - back * 136 + 80;
      const unsigned pl = pack2(lg[j] * inv, 0.f), ph = pl << 16;
#pragma unroll
      for (int i = 0; i < 5; ++i) {
        const u32x2 a = *LDS_PTR(const u32x2, vr + i * 8);
        const unsigned x0 = a[0], x1 = a[1];
        o[4 * i + 0] = dot2(x0, pl, o[4 * i + 0]);
        o[4 * i + 1] = dot2(x0, ph, o[4 * i + 1]);
        o[4 * i + 2] = dot2(x1, pl, o[4 * i + 2]);
        o[4 * i + 3] = dot2(x1, ph, o[4 * i + 3]);
      }
    }
    char* og = ws + WS_O + (size_t)s * (D * 2) + hA * 40;
#pragma unroll
    for (int i = 0; i < 5; ++i) *(u32x2*)(og + i * 8) = (u32x2){pack2(o[4 * i], o[4 * i + 1]), pack2(o[4 * i + 2], o[4 * i + 3])};
    return;
  }

  const int xcd = blockIdx.x & 7, slot = blockIdx.x >> 3;
  const int head = xcd * 4 + (slot >> 3), qc = slot & 7;
  const int qg = w & 3, kh = w >> 2;
  const int r32 = lane & 31, h = lane >> 5;
  const char* kvh = ws + WS_KV + (size_t)head * 64 * 3072;

  const char* dsrc[3];
  int ddst[3];
#pragma unroll
  for (int j = 0; j < 3; ++j) {
    const int id = w * 3 + j, half = id / 12, off = (id - half * 12) * 1024;
    dsrc[j] = kvh + (size_t)half * 32 * 3072 + off + lane * 16;
    ddst[j] = half * 12288 + off;
  }
  auto stage = [&](int i) {
    char* b = lds + (i % 3) * K2_STAGE;
#pragma unroll
    for (int j = 0; j < 3; ++j) glds16(dsrc[j] + (size_t)i * 12288, b + ddst[j]);
  };
  stage(0);
  stage(1);

  bf16x8 qa0, qa1, qb0, qb1;
  {
    const char* qg_ = ws + WS_QB + ((size_t)head * 64 + qc * 8 + qg * 2) * 1536;
    qa0 = *(const bf16x8*)(qg_ + h * 512 + r32 * 16);
    qa1 = *(const bf16x8*)(qg_ + 1024 + r32 * 16);
    qb0 = *(const bf16x8*)(qg_ + 1536 + h * 512 + r32 * 16);
    qb1 = *(const bf16x8*)(qg_ + 1536 + 1024 + r32 * 16);
    float kn = ((const float*)(ws + WS_KN))[head * 32 + r32];
#pragma unroll
    for (int o = 16; o; o >>= 1) kn = fmaxf(kn, __shfl_xor(kn, o));
    if (h) { qa1 = (bf16x8){}; qb1 = (bf16x8){}; }
    float na = sumsq8(qa0) + sumsq8(qa1), nb = sumsq8(qb0) + sumsq8(qb1);
    na += swap32(na);
    nb += swap32(nb);
    const float ra = sqrtf(na * kn) * 1.01f - 31.f, rb = sqrtf(nb * kn) * 1.01f - 31.f;
    if (!h) {
      qa1[4] = (short)(pack2(-ra, 0.f) & 0xffff);
      qb1[4] = (short)(pack2(-rb, 0.f) & 0xffff);
    }
  }
  const int dv = r32 < 24 ? r32 : (r32 < 28 ? r32 - 4 : r32 - 12);
  const int koff0 = kh * 12288 + h * 512 + r32 * 16, koff1 = kh * 12288 + 1024 + r32 * 16;
  const int voff0 = kh * 12288 + 1536 + h * 384 + dv * 16, voff1 = voff0 + 768;

  f32x16 Oa = {}, Ob = {}, Sa, Sb;
  const f32x16 zero = {};
  WAIT_VM(3);
  RAW_BAR();
  bf16x8 k0 = *LDS_PTR(const bf16x8, lds + koff0), k1 = *LDS_PTR(const bf16x8, lds + koff1);
  Sa = MFMA32(k0, qa0, zero);
  Sa = MFMA32(k1, qa1, Sa);
#pragma unroll 1
  for (int i = 0; i < 8; ++i) {
    const char* b = lds + (i % 3) * K2_STAGE;
    const char* bn = lds + ((i + 1) % 3) * K2_STAGE;
#pragma unroll
    for (int bb = 0; bb < 4; ++bb) {
      const bf16x8 v0 = *LDS_PTR(const bf16x8, b + bb * 3072 + voff0), v1 = *LDS_PTR(const bf16x8, b + bb * 3072 + voff1);
      Sb = MFMA32(k0, qb0, zero);
      Sb = MFMA32(k1, qb1, Sb);
      if (bb == 3) {
        WAIT_VM(0);
        RAW_BAR();
        if (i + 2 < 8) stage(i + 2);
        k0 = *LDS_PTR(const bf16x8, bn + koff0);
        k1 = *LDS_PTR(const bf16x8, bn + koff1);
      } else {
        k0 = *LDS_PTR(const bf16x8, b + (bb + 1) * 3072 + koff0);
        k1 = *LDS_PTR(const bf16x8, b + (bb + 1) * 3072 + koff1);
      }
      bf16x8 p0, p1;
      exp_pack(Sa, p0, p1);
      Oa = MFMA32(v0, p0, Oa);
      Oa = MFMA32(v1, p1, Oa);
      Sa = MFMA32(k0, qa0, zero);
      Sa = MFMA32(k1, qa1, Sa);
      exp_pack(Sb, p0, p1);
      Ob = MFMA32(v0, p0, Ob);
      Ob = MFMA32(v1, p1, Ob);
    }
  }
  RAW_BAR();
  {
    float* ex = (float*)lds + (size_t)((qg * 2 + kh) * 16) * 64 + lane;
#pragma unroll
    for (int r = 0; r < 16; ++r) *LDS_PTR(float, ex + r * 64) = kh ? Oa[r] : Ob[r];
  }
  __syncthreads();
  f32x16 O;
  {
    const float* ex = (const float*)lds + (size_t)((qg * 2 + (kh ^ 1)) * 16) * 64 + lane;
#pragma unroll
    for (int r = 0; r < 16; ++r) O[r] = (kh ? Ob[r] : Oa[r]) + *LDS_PTR(const float, ex + r * 64);
  }
  float l = h ? O[8] : 0.f;
  l += swap32(l);
  float amax = fabsf(l);
#pragma unroll
  for (int r = 0; r < 12; ++r) amax = fmaxf(amax, fabsf(O[r]));
  const bool bad = !(l >= 8.4703294725430034e-22f  ) || !(amax <= 3.0e38f);
  if (__any(bad)) {
    bf16x8 f0 = kh ? qb0 : qa0, f1 = kh ? qb1 : qa1;
    if (!h) f1[4] = 0;
    attn_fallback(kvh, f0, f1, lane, O);
    l = h ? O[8] : 0.f;
    l += swap32(l);
  }
  const float inv = 1.f / l;
  const int q = (qc * 8 + qg * 2 + kh) * 32 + r32;
  char* og = ws + WS_O + ((size_t)S + q) * (D * 2) + head * 40 + h * 8;
#pragma unroll
  for (int g = 0; g < 3; ++g) {
    if (g < 2 || !h)
      *(u32x2*)(og + g * 16) = (u32x2){pack2(O[4 * g] * inv, O[4 * g + 1] * inv), pack2(O[4 * g + 2] * inv, O[4 * g + 3] * inv)};
  }
}

constexpr int K3_XB = 128 * 128, K3_WB = 64 * 160, K3_STAGE = K3_XB + K3_WB;
constexpr int K3_NBUF = 4;
constexpr int K3_LDS = K3_NBUF * K3_STAGE + 256;

__global__ __launch_bounds__(512, 2) void k3_proj(const char* __restrict__ ws, const unsigned short* __restrict__ woA,
                                                  const unsigned short* __restrict__ woB, unsigned short* __restrict__ out) {
  extern __shared__ __attribute__((aligned(16))) char lds[];
  const int tid = threadIdx.x, lane = tid & 63;
  const int w = __builtin_amdgcn_readfirstlane(tid >> 6);
  const int xcd = blockIdx.x & 7, slot = blockIdx.x >> 3;
  const int prob = xcd >> 2, mblk = (xcd & 3) * 4 + (slot >> 3), nblk = slot & 7;
  const char* X = ws + WS_O + (size_t)prob * S * D * 2;
  const char* W = (const char*)(prob ? woB : woA);
  const int m0 = mblk * 128, n0 = nblk * 80;

  const char* src[4];
  int dst[4];
#pragma unroll
  for (int j = 0; j < 2; ++j) {
    const int i = w + 8 * j, row = i * 8 + (lane >> 3), c = (lane & 7) ^ (row & 7);
    src[j] = X + (size_t)(m0 + row) * (D * 2) + c * 16;
    dst[j] = i * 1024;
  }
#pragma unroll
  for (int j = 2; j < 4; ++j) {
    int iw = w + 8 * (j - 2);
    if (iw >= 10) iw -= 8;
    const int idx = iw * 64 + lane, rho = idx / 10, c10 = idx - rho * 10;
    const int k = (rho & ~12) | ((rho & 4) << 1) | ((rho & 8) >> 1);
    src[j] = W + (size_t)k * (D * 2) + n0 * 2 + c10 * 16;
    dst[j] = K3_XB + iw * 1024;
  }
  auto stage = [&](int kt) {
    char* b = lds + (kt % K3_NBUF) * K3_STAGE;
#pragma unroll
    for (int j = 0; j < 2; ++j) glds16(src[j] + kt * 128, b + dst[j]);
#pragma unroll
    for (int j = 2; j < 4; ++j) glds16(src[j] + (size_t)kt * (64 * D * 2), b + dst[j]);
  };
  const int wm = w >> 1, wn = w & 1;
  const int l15 = lane & 15, kq = lane >> 4;
  int xoff[2][2];
#pragma unroll
  for (int mb = 0; mb < 2; ++mb) {
    const int row = wm * 32 + mb * 16 + l15;
#pragma unroll
    for (int ks = 0; ks < 2; ++ks) xoff[mb][ks] = row * 128 + (((ks * 4 + kq) ^ (row & 7)) << 4);
  }
  const int woff = K3_XB + ((kq >> 1) * 16 + (kq & 1) * 4 + (l15 >> 2)) * 160 + (l15 & 3) * 8 + wn * 96;
  f32x4 acc[3][2];
#pragma unroll
  for (int a = 0; a < 3; ++a)
#pragma unroll
    for (int b = 0; b < 2; ++b) acc[a][b] = (f32x4){0.f, 0.f, 0.f, 0.f};

  stage(0); stage(1); stage(2);
#pragma unroll 1
  for (int kt = 0; kt < 10; ++kt) {
    if (kt < 8) WAIT_VM(8); else if (kt == 8) WAIT_VM(4); else WAIT_VM(0);
    RAW_BAR();
    if (kt + 3 < 10) stage(kt + 3);
    const char* b = lds + (kt % K3_NBUF) * K3_STAGE;
#pragma unroll
    for (int ks = 0; ks < 2; ++ks) {
      bf16x8 xf[2];
#pragma unroll
      for (int mb = 0; mb < 2; ++mb) xf[mb] = *LDS_PTR(const bf16x8, b + xoff[mb][ks]);
#pragma unroll
      for (int nb = 0; nb < 3; ++nb) {
        const bf16x4 lo = tr_read(b + woff + ks * (32 * 160) + nb * 32);
        const bf16x4 hi = tr_read(b + woff + ks * (32 * 160) + 8 * 160 + nb * 32);
        const bf16x8 wf = __builtin_shufflevector(lo, hi, 0, 1, 2, 3, 4, 5, 6, 7);
#pragma unroll
        for (int mb = 0; mb < 2; ++mb) acc[nb][mb] = __builtin_amdgcn_mfma_f32_16x16x32_bf16(wf, xf[mb], acc[nb][mb], 0, 0, 0);
      }
    }
  }
#pragma unroll
  for (int nb = 0; nb < 3; ++nb) {
    if (wn * 3 + nb < 5) {
#pragma unroll
      for (int mb = 0; mb < 2; ++mb) {
        const int m = m0 + wm * 32 + mb * 16 + l15, n = n0 + (wn * 3 + nb) * 16 + kq * 4;
        const f32x4 v = acc[nb][mb];
        *(u32x2*)(out + (size_t)m * (2 * D) + prob * D + n) = (u32x2){pack2(v[0], v[1]), pack2(v[2], v[3])};
      }
    }
  }
}

extern "C" void kernel_launch(void* const* d_in, const int* in_sizes, int n_in,
                              void* d_out, int out_size, void* d_ws, size_t ws_size,
                              hipStream_t stream) {
  (void)in_sizes; (void)n_in; (void)out_size; (void)ws_size;
  const unsigned short* xA = (const unsigned short*)d_in[0];
  const unsigned short* wA = (const unsigned short*)d_in[1];
  const unsigned short* oA = (const unsigned short*)d_in[2];
  const unsigned short* xB = (const unsigned short*)d_in[3];
  const unsigned short* wB = (const unsigned short*)d_in[4];
  const unsigned short* oB = (const unsigned short*)d_in[5];
  char* ws = (char*)d_ws;
  (void)hipFuncSetAttribute((const void*)k1_qkv, hipFuncAttributeMaxDynamicSharedMemorySize, K1_LDS);
  (void)hipFuncSetAttribute((const void*)k2_attn, hipFuncAttributeMaxDynamicSharedMemorySize, K2_LDS);
  (void)hipFuncSetAttribute((const void*)k3_proj, hipFuncAttributeMaxDynamicSharedMemorySize, K3_LDS);
  hipLaunchKernelGGL(k1_qkv, dim3(256), dim3(512), K1_LDS, stream, xA, wA, xB, wB, ws);
  hipLaunchKernelGGL(k2_attn, dim3(256), dim3(768), K2_LDS, stream, ws);
  hipLaunchKernelGGL(k3_proj, dim3(256), dim3(512), K3_LDS, stream, (const char*)ws, oA, oB, (unsigned short*)d_out);
}
```

```cpp
#include <hip/hip_runtime.h>
#include <hip/hip_bf16.h>
#include <stdint.h>

typedef __attribute__((ext_vector_type(8))) short bf16x8;
typedef __attribute__((ext_vector_type(4))) short bf16x4;
typedef __attribute__((ext_vector_type(4))) float f32x4;
typedef __attribute__((ext_vector_type(2))) float f32x2;
typedef __attribute__((ext_vector_type(16))) float f32x16;
typedef __attribute__((ext_vector_type(4))) unsigned u32x4;
typedef __attribute__((ext_vector_type(2))) unsigned u32x2;
typedef __attribute__((ext_vector_type(2))) __bf16 bf16x2_t;
typedef __attribute__((address_space(3))) const char* lds_cptr;
typedef short v4i16_t __attribute__((ext_vector_type(4)));

#define DI __device__ __forceinline__
#define LDS_PTR(T, p) ((__attribute__((address_space(3))) T*)(p))

constexpr int S = 2048, D = 640, N3 = 1920, NH = 32, DH = 20;
constexpr float SM_SCALE = 0.22360679774997896f;
constexpr float LOG2E = 1.4426950408889634f;
constexpr float QSCALE = SM_SCALE * LOG2E;

constexpr size_t WS_QB = 0;
constexpr size_t WS_KV = WS_QB + (size_t)NH * 64 * 1536;
constexpr size_t WS_KN = WS_KV + (size_t)NH * 64 * 3072;
constexpr size_t WS_O = WS_KN + 8192;
constexpr size_t WS_AQ = WS_O + (size_t)2 * S * D * 2;
constexpr size_t WS_END = WS_AQ + (size_t)NH * 15 * S * 8 + 4096;

DI unsigned pack2(float lo, float hi) {
  f32x2 v = {lo, hi};
  return __builtin_bit_cast(unsigned, __builtin_convertvector(v, bf16x2_t));
}
DI float rowmax16(float x) {
  x = fmaxf(x, __int_as_float(__builtin_amdgcn_update_dpp(0, __float_as_int(x), 0x111, 0xf, 0xf, true)));
  x = fmaxf(x, __int_as_float(__builtin_amdgcn_update_dpp(0, __float_as_int(x), 0x112, 0xf, 0xf, true)));
  x = fmaxf(x, __int_as_float(__builtin_amdgcn_update_dpp(0, __float_as_int(x), 0x114, 0xf, 0xf, true)));
  x = fmaxf(x, __int_as_float(__builtin_amdgcn_update_dpp(0, __float_as_int(x), 0x118, 0xf, 0xf, true)));
  return x;
}
DI float lanef(float x, int l) { return __int_as_float(__builtin_amdgcn_readlane(__float_as_int(x), l)); }
DI float dot2(unsigned a, unsigned b, float c) {
  return __builtin_amdgcn_fdot2_f32_bf16(__builtin_bit_cast(bf16x2_t, a), __builtin_bit_cast(bf16x2_t, b), c, false);
}
DI float bf_lo(unsigned u) { return __uint_as_float(u << 16); }
DI float bf_hi(unsigned u) { return __uint_as_float(u & 0xffff0000u); }
DI void glds16(const char* base, unsigned voff, unsigned lds_a) {
  unsigned keep;
  const uint64_t b64 = (uint64_t)base;
  const uint64_t sbase = ((uint64_t)(unsigned)__builtin_amdgcn_readfirstlane((unsigned)(b64 >> 32)) << 32) |
                         (unsigned)__builtin_amdgcn_readfirstlane((unsigned)b64);
  const unsigned lds_addr = __builtin_amdgcn_readfirstlane(lds_a);
  asm volatile("s_mov_b32 %0, m0\n\ts_mov_b32 m0, %3\n\ts_nop 0\n\tglobal_load_lds_dwordx4 %1, %2\n\ts_mov_b32 m0, %0"
               : "=&s"(keep) : "v"(voff), "s"(sbase), "s"(lds_addr) : "memory");
}
#define SBAR() __builtin_amdgcn_sched_barrier(0)
#define PIN(x) asm volatile("" : "+v"(x))
#define KEEP(x) asm volatile("" :: "v"(x))
DI bf16x4 tr_read(const char* p) {
  return __builtin_bit_cast(bf16x4, __builtin_amdgcn_ds_read_tr16_b64_v4i16(LDS_PTR(v4i16_t, p)));
}
#define TL(slot) do {} while (0)
#define TL_DUMP() do {} while (0)
#define STAMP_AT(kid, idx) do {} while (0)
#define ST_NT 1
#define ST16(p, v) __builtin_nontemporal_store((u32x4)(v), (u32x4*)(p))
#define WAIT_VM(n) asm volatile("s_waitcnt vmcnt(" #n ")" ::: "memory")
#define WAIT_LGKM0() __builtin_amdgcn_s_waitcnt(0xC07F)
#define RAW_BAR() do { asm volatile("" ::: "memory"); WAIT_LGKM0(); __builtin_amdgcn_s_barrier(); asm volatile("" ::: "memory"); } while (0)

template <int BM, int NBUF>
DI void gemm_loader(const char* X, const char* W, unsigned ldw, int rot, char* lds, int w, int lane) {
  constexpr int XB = BM * 128, STAGE = XB + 64 * 256, NX = BM / 32, NP = NX + 4;
  __builtin_amdgcn_s_setprio(3);
  unsigned src[NP], dst[NP];
  const unsigned lds0 = (unsigned)(uintptr_t)lds;
#pragma unroll
  for (int j = 0; j < NX; ++j) {
    const int i = w + 4 * j, row = i * 8 + (lane >> 3), c = (lane & 7) ^ (row & 7);
    src[j] = row * (D * 2) + c * 16;
    dst[j] = lds0 + i * 1024;
  }
#pragma unroll
  for (int j = 0; j < 4; ++j) {
    const int i = w + 4 * j, rho = i * 4 + (lane >> 4), c16 = lane & 15;
    const int k = (rho & ~12) | ((rho & 4) << 1) | ((rho & 8) >> 1);
    src[NX + j] = k * ldw + (((c16 >> 1) ^ (rho & 7)) << 5) + (c16 & 1) * 16;
    dst[NX + j] = lds0 + XB + i * 1024;
  }
  auto stage = [&](int kt, int buf) {
    int k = kt + rot; if (k >= 10) k -= 10;
    const char* xs = X + k * 128;
    const char* wsrc = W + (size_t)k * 64 * ldw;
#pragma unroll
    for (int j = 0; j < 4; ++j) glds16(wsrc, src[NX + j], dst[NX + j] + buf * STAGE);
#pragma unroll
    for (int j = 0; j < NX; ++j) glds16(xs, src[j], dst[j] + buf * STAGE);
  };
  auto wait_stages = [&](int n) {
    if (n <= 0) WAIT_VM(0);
    else if (n == 1) { if (NP == 12) WAIT_VM(12); else WAIT_VM(8); }
    else if (n == 2) { if (NP == 12) WAIT_VM(24); else WAIT_VM(16); }
    else { if (NP == 12) WAIT_VM(36); else WAIT_VM(24); }
  };
#pragma unroll
  for (int i = 0; i < NBUF; ++i) stage(i, i);
  TL(0);
  wait_stages(NBUF - 1);
  TL(1);
  __builtin_amdgcn_s_barrier();
  int buf = 0;
#pragma unroll 1
  for (int kt = 0; kt < 10; ++kt) {
    wait_stages((kt + NBUF - 1 < 9 ? kt + NBUF - 1 : 9) - (kt + 1));
    TL(2 + 3 * kt);
    __builtin_amdgcn_s_barrier();
    TL(3 + 3 * kt);
    if (kt + NBUF < 10) stage(kt + NBUF, buf);
    TL(4 + 3 * kt);
    buf = buf == NBUF - 1 ? 0 : buf + 1;
  }
  TL_DUMP();
}

template <int BM, int NBUF, bool SWAP>
DI void gemm_core(char* lds, int w, int lane, f32x4 (&acc)[4][BM / 64]) {
  constexpr int MB = BM / 64, XB = BM * 128, STAGE = XB + 64 * 256;
  const int wm = w >> 1, wn = w & 1, l15 = lane & 15, kq = lane >> 4;
  int xoff[MB][2], woff[4];
#pragma unroll
  for (int mb = 0; mb < MB; ++mb) {
    const int row = wm * (BM / 4) + mb * 16 + l15;
#pragma unroll
    for (int ks = 0; ks < 2; ++ks) xoff[mb][ks] = row * 128 + (((ks * 4 + kq) ^ (row & 7)) << 4);
  }
  {
    const int r7 = (kq & 1) * 4 + (l15 >> 2);
#pragma unroll
    for (int nb = 0; nb < 4; ++nb) woff[nb] = XB + ((kq >> 1) * 16 + r7) * 256 + (((wn * 4 + nb) ^ r7) << 5) + (l15 & 3) * 8;
  }
#pragma unroll
  for (int a = 0; a < 4; ++a)
#pragma unroll
    for (int b = 0; b < MB; ++b) acc[a][b] = (f32x4){0.f, 0.f, 0.f, 0.f};

  bf16x8 xf[2][MB], wf[2][4];
  auto rd = [&](const char* b, int ks) {
#pragma unroll
    for (int mb = 0; mb < MB; ++mb) xf[ks][mb] = *LDS_PTR(const bf16x8, b + xoff[mb][ks]);
#pragma unroll
    for (int nb = 0; nb < 4; ++nb) {
      const bf16x4 lo = tr_read(b + woff[nb] + ks * (32 * 256));
      const bf16x4 hi = tr_read(b + woff[nb] + ks * (32 * 256) + 8 * 256);
      wf[ks][nb] = __builtin_shufflevector(lo, hi, 0, 1, 2, 3, 4, 5, 6, 7);
    }
  };
  auto mm = [&](int ks) {
#pragma unroll
    for (int nb = 0; nb < 4; ++nb)
#pragma unroll
      for (int mb = 0; mb < MB; ++mb)
        acc[nb][mb] = SWAP ? __builtin_amdgcn_mfma_f32_16x16x32_bf16(xf[ks][mb], wf[ks][nb], acc[nb][mb], 0, 0, 0)
                           : __builtin_amdgcn_mfma_f32_16x16x32_bf16(wf[ks][nb], xf[ks][mb], acc[nb][mb], 0, 0, 0);
  };
  TL(0);
  auto interleave = [&]() {
#pragma unroll
    for (int i = 0; i < 4 * MB; ++i) {
      __builtin_amdgcn_sched_group_barrier(0x008, 1, 0);
      __builtin_amdgcn_sched_group_barrier(0x100, MB == 4 ? 1 : 2, 0);
    }
  };
  RAW_BAR();
  TL(1);
  rd(lds, 0);
  int buf = 0;
#pragma unroll 1
  for (int kt = 0; kt < 10; ++kt) {
    const char* b = lds + buf * STAGE;
    const int nbuf = buf == NBUF - 1 ? 0 : buf + 1;
    WAIT_LGKM0();
    SBAR();
    rd(b, 1);
    mm(0);
    interleave();
    SBAR();
    TL(2 + 3 * kt);
    RAW_BAR();
    TL(3 + 3 * kt);
    rd(lds + nbuf * STAGE, 0);
    mm(1);
    interleave();
    SBAR();
    TL(4 + 3 * kt);
    buf = nbuf;
  }
  TL_DUMP();
}

constexpr int K1_STAGE = 256 * 128 + 64 * 256;
constexpr int K1_LDS = 3 * K1_STAGE;

__global__ __launch_bounds__(768, 3) void k1_qkv(const unsigned short* __restrict__ xA, const unsigned short* __restrict__ wA,
                                                 const unsigned short* __restrict__ xB, const unsigned short* __restrict__ wB,
                                                 char* __restrict__ ws) {
  extern __shared__ __attribute__((aligned(16))) char lds[];
  const int tid = threadIdx.x, lane = tid & 63;
  const int w = __builtin_amdgcn_readfirstlane(tid >> 6);
  const int xcd = blockIdx.x & 7, slot = blockIdx.x >> 3;
  const int mloc = slot / 15, nblk = slot - mloc * 15;
  const int prob = xcd >> 2, mblk = (xcd & 3) * 2 + mloc;
  const int rot = (nblk + 5 * mloc) % 10;
  const char* X = (const char*)(prob ? xB : xA) + (size_t)mblk * 256 * (D * 2);
  const char* W = (const char*)(prob ? wB : wA) + nblk * 256;
  const int seg = nblk / 5;
  const bool swap = prob && seg == 2;
  STAMP_AT(0, 0);
  if (w >= 8) { gemm_loader<256, 3>(X, W, N3 * 2, rot, lds, w - 8, lane); STAMP_AT(0, 4); return; }
  f32x4 acc[4][4];
  if (swap) gemm_core<256, 3, true>(lds, w, lane, acc);
  else gemm_core<256, 3, false>(lds, w, lane, acc);
  STAMP_AT(0, 2);

  const int wm = w >> 1, wn = w & 1, l15 = lane & 15, kq = lane >> 4;
  const int c0 = (nblk - seg * 5) * 128 + wn * 64;
  const int row0 = mblk * 256 + wm * 64;
  char* st = lds + w * 8192;
  if (!swap) {
    const float sc = (prob && seg == 0) ? QSCALE : 1.f;
#pragma unroll
    for (int nb = 0; nb < 4; ++nb)
#pragma unroll
      for (int mb = 0; mb < 4; ++mb) {
        const f32x4 v = acc[nb][mb] * sc;
        *LDS_PTR(u32x2, st + (nb * 4 + kq) * 512 + (mb * 16 + l15) * 8) = (u32x2){pack2(v[0], v[1]), pack2(v[2], v[3])};
      }
    asm volatile("s_waitcnt lgkmcnt(0)" ::: "memory");
    const int tp = lane & 31;
#pragma unroll
    for (int it = 0; it < 8; ++it) {
      const u32x4 v = *LDS_PTR(const u32x4, st + it * 1024 + lane * 16);
      const int q = (c0 >> 2) + it * 2 + (lane >> 5), hh = q / 5, dq = q - hh * 5;
      if (!prob) {
        ST16(ws + WS_AQ + ((size_t)(hh * 15 + seg * 5 + dq) * S + row0 + 2 * tp) * 8, v);
      } else {
        char* g = ws + (seg ? WS_KV : WS_QB) + ((size_t)hh * 64 + (row0 >> 5) + (tp >> 4)) * (seg ? 3072 : 1536) + dq * 256 + (tp & 15) * 16;
        ST16(g, v);
        if (seg && dq == 4) *(u32x4*)(g + 256) = (u32x4){0x3F80u, 0u, 0x3F80u, 0u};
      }
    }
    if (prob && seg == 1) {
      float* kn = (float*)(ws + WS_KN) + (row0 >> 6);
      u32x2 qv[16];
#pragma unroll
      for (int q = 0; q < 16; ++q) qv[q] = *LDS_PTR(const u32x2, st + q * 512 + lane * 8);
      float s = 0.f;
      int hcur = c0 / 20;
#pragma unroll
      for (int q = 0; q < 16; ++q) {
        const unsigned u0 = qv[q][0], u1 = qv[q][1];
        float x;
        x = bf_lo(u0); s = fmaf(x, x, s); x = bf_hi(u0); s = fmaf(x, x, s);
        x = bf_lo(u1); s = fmaf(x, x, s); x = bf_hi(u1); s = fmaf(x, x, s);
        const bool hend = c0 + q * 4 + 4 == (hcur + 1) * 20;
        if (hend || q == 15) {
          s = rowmax16(s);
          s = fmaxf(fmaxf(lanef(s, 15), lanef(s, 31)), fmaxf(lanef(s, 47), lanef(s, 63)));
          const bool first = hcur * 20 >= c0;
          if (lane == 0) {
            kn[(hcur * 2 + (first ? 0 : 1)) * 32] = s;
            if (first && hend) kn[(hcur * 2 + 1) * 32] = 0.f;
          }
          s = 0.f; ++hcur;
        }
      }
    }
  } else {
#pragma unroll
    for (int nb = 0; nb < 4; ++nb)
#pragma unroll
      for (int mb = 0; mb < 4; ++mb) {
        const f32x4 v = acc[nb][mb];
        *LDS_PTR(u32x2, st + (mb * 2 + (kq & 1)) * 1024 + (nb * 16 + l15) * 16 + (kq >> 1) * 8) = (u32x2){pack2(v[0], v[1]), pack2(v[2], v[3])};
      }
    asm volatile("s_waitcnt lgkmcnt(0)" ::: "memory");
    const int c = c0 + lane, hh = c / 20, d = c - hh * 20;
    char* g = ws + WS_KV + ((size_t)hh * 64 + (row0 >> 5)) * 3072 + 1536 + d * 16;
#pragma unroll
    for (int it = 0; it < 8; ++it) {
      const u32x4 v = *LDS_PTR(const u32x4, st + it * 1024 + lane * 16);
      ST16(g + (it >> 2) * 3072 + (it & 3) * 384, v);
    }
#pragma unroll
    for (int it = 0; it < 2; ++it) {
      const int h2 = (c0 + 19) / 20 + it * 2 + (lane >> 5), blk = (lane >> 4) & 1, sh = (lane >> 2) & 3, d2 = 20 + (lane & 3);
      const unsigned o = d2 == 20 ? 0x3F803F80u : 0u;
      if (h2 * 20 < c0 + 64)
        *(u32x4*)(ws + WS_KV + ((size_t)h2 * 64 + (row0 >> 5) + blk) * 3072 + 1536 + sh * 384 + d2 * 16) = (u32x4){o, o, o, o};
    }
  }
  STAMP_AT(0, 4);
}

constexpr int K2_STAGE = 2 * 4 * 3072;
constexpr int K2_LOC = 15 * 640;
constexpr int K2_LDS = 3 * K2_STAGE + 4 * K2_LOC;
#define MFMA32(a, b, c) __builtin_amdgcn_mfma_f32_32x32x16_bf16(a, b, c, 0, 0, 0)

DI bf16x8 cat44(bf16x4 a, bf16x4 b) { return __builtin_shufflevector(a, b, 0, 1, 2, 3, 4, 5, 6, 7); }
DI void exp_pack(const f32x16& s, bf16x8& p0, bf16x8& p1) {
  u32x4 a, b;
#pragma unroll
  for (int i = 0; i < 4; ++i) {
    a[i] = pack2(__builtin_amdgcn_exp2f(s[2 * i]), __builtin_amdgcn_exp2f(s[2 * i + 1]));
    b[i] = pack2(__builtin_amdgcn_exp2f(s[8 + 2 * i]), __builtin_amdgcn_exp2f(s[8 + 2 * i + 1]));
  }
  p0 = __builtin_bit_cast(bf16x8, a);
  p1 = __builtin_bit_cast(bf16x8, b);
}
DI float swap32(float x) {
  const auto r = __builtin_amdgcn_permlane32_swap(__float_as_uint(x), __float_as_uint(x), false, false);
  return __uint_as_float((threadIdx.x & 32) ? r[0] : r[1]);
}
DI float sumsq8(bf16x8 f) {
  const u32x4 u = __builtin_bit_cast(u32x4, f);
  float s = 0.f;
#pragma unroll
  for (int i = 0; i < 4; ++i) { float x = bf_lo(u[i]); s = fmaf(x, x, s); x = bf_hi(u[i]); s = fmaf(x, x, s); }
  return s;
}

DI void attn_fallback(const char* kvh, bf16x8 q0, bf16x8 q1, int lane, f32x16& O) {
  const int r32 = lane & 31, h = lane >> 5;
  const int dv = r32 < 24 ? r32 : (r32 < 28 ? r32 - 4 : r32 - 12);
  float m = -INFINITY;
  O = (f32x16){};
#pragma unroll 1
  for (int t = 0; t < 64; ++t) {
    const char* g = kvh + (size_t)t * 3072;
    const bf16x8 k0 = cat44(*(const bf16x4*)(g + h * 512 + r32 * 8), *(const bf16x4*)(g + h * 512 + 256 + r32 * 8));
    const bf16x8 k1 = cat44(*(const bf16x4*)(g + 1024 + r32 * 8), *(const bf16x4*)(g + 1280 + r32 * 8));
    const bf16x8 v0 = *(const bf16x8*)(g + 1536 + h * 384 + dv * 16);
    const bf16x8 v1 = *(const bf16x8*)(g + 1536 + 768 + h * 384 + dv * 16);
    f32x16 s = MFMA32(k0, q0, (f32x16){});
    s = MFMA32(k1, q1, s);
    float pm = s[0];
#pragma unroll
    for (int i = 1; i < 16; ++i) pm = fmaxf(pm, s[i]);
    pm = fmaxf(pm, swap32(pm));
    const float mn = fmaxf(m, pm);
    const float alpha = __builtin_amdgcn_exp2f(m - mn);
    m = mn;
#pragma unroll
    for (int i = 0; i < 16; ++i) { s[i] -= mn; O[i] *= alpha; }
    bf16x8 p0, p1;
    exp_pack(s, p0, p1);
    O = MFMA32(v0, p0, O);
    O = MFMA32(v1, p1, O);
  }
}

template <bool MASK>
DI void local_attn(const char* me, int s, float (&o)[20]) {
  u32x2 q[5], kv[8][5];
#pragma unroll
  for (int i = 0; i < 5; ++i) q[i] = *LDS_PTR(const u32x2, me + i * 640);
#pragma unroll
  for (int j = 0; j < 8; ++j) {
    const char* kr = me - (MASK ? min(2 * j, s) : 2 * j) * 8 + 5 * 640;
#pragma unroll
    for (int i = 0; i < 5; ++i) kv[j][i] = *LDS_PTR(const u32x2, kr + i * 640);
  }
  SBAR();
  float lg[8];
  float mx = -INFINITY;
#pragma unroll
  for (int j = 0; j < 8; ++j) {
    float a0 = 0.f, a1 = 0.f;
#pragma unroll
    for (int i = 0; i < 5; ++i) {
      const unsigned x0 = kv[j][i][0], x1 = kv[j][i][1], y0 = q[i][0], y1 = q[i][1];
      a0 = dot2(x0, y0, a0);
      a1 = dot2(x1, y1, a1);
    }
    lg[j] = (a0 + a1) * QSCALE;
    if (MASK && s < 2 * j) lg[j] = -INFINITY;
    mx = fmaxf(mx, lg[j]);
  }
  SBAR();
#pragma unroll
  for (int j = 0; j < 8; ++j) {
    const char* vr = me - (MASK ? min(2 * j, s) : 2 * j) * 8 + 10 * 640;
#pragma unroll
    for (int i = 0; i < 5; ++i) kv[j][i] = *LDS_PTR(const u32x2, vr + i * 640);
  }
  SBAR();
  float sum = 0.f;
#pragma unroll
  for (int j = 0; j < 8; ++j) { lg[j] = __builtin_amdgcn_exp2f(lg[j] - mx); sum += lg[j]; }
  const float inv = 1.f / sum;
#pragma unroll
  for (int i = 0; i < 20; ++i) o[i] = 0.f;
#pragma unroll
  for (int j = 0; j < 8; ++j) {
    const unsigned pl = pack2(lg[j] * inv, 0.f), ph = pl << 16;
#pragma unroll
    for (int i = 0; i < 5; ++i) {
      const unsigned x0 = kv[j][i][0], x1 = kv[j][i][1];
      o[4 * i + 0] = dot2(x0, pl, o[4 * i + 0]);
      o[4 * i + 1] = dot2(x0, ph, o[4 * i + 1]);
      o[4 * i + 2] = dot2(x1, pl, o[4 * i + 2]);
      o[4 * i + 3] = dot2(x1, ph, o[4 * i + 3]);
    }
  }
}

__global__ __launch_bounds__(768, 3) void k2_attn(char* __restrict__ ws) {
  extern __shared__ __attribute__((aligned(16))) char lds[];
  const int tid = threadIdx.x, lane = tid & 63;
  const int w = __builtin_amdgcn_readfirstlane(tid >> 6);

  STAMP_AT(1, 0);
  if (w >= 8) {
    __builtin_amdgcn_s_setprio(3);
    const int task = blockIdx.x * 4 + (w - 8), hA = task >> 5, r0 = (task & 31) * 64;
    char* L = lds + 3 * K2_STAGE + (w - 8) * K2_LOC;
    const char* g = ws + WS_AQ + ((ptrdiff_t)hA * 15 * S + r0 - 16) * 8;
    __builtin_amdgcn_s_barrier();
    if (lane < 40) {
#pragma unroll
      for (int i = 0; i < 15; ++i) glds16(g + (size_t)i * S * 8, lane * 16, (unsigned)(uintptr_t)L + i * 640);
    }
    __builtin_amdgcn_s_barrier();
    WAIT_VM(0);
    STAMP_AT(1, 1);
    const int s = r0 + lane;
    float o[20];
    if (r0 == 0) local_attn<true>(L + (lane + 16) * 8, s, o); else local_attn<false>(L + (lane + 16) * 8, s, o);
    char* og = ws + WS_O + (size_t)s * (D * 2) + hA * 40;
#pragma unroll
    for (int i = 0; i < 5; ++i) *(u32x2*)(og + i * 8) = (u32x2){pack2(o[4 * i], o[4 * i + 1]), pack2(o[4 * i + 2], o[4 * i + 3])};
    STAMP_AT(1, 4);
    return;
  }

  const int xcd = blockIdx.x & 7, slot = blockIdx.x >> 3;
  const int head = xcd * 4 + (slot >> 3), qc = slot & 7;
  const int qg = w & 3, kh = w >> 2;
  const int r32 = lane & 31, h = lane >> 5;
  const char* kvh = ws + WS_KV + (size_t)head * 64 * 3072;

  const int dhalf = w >> 2, doff = (w & 3) * 3072;
  const char* dsrc = kvh + (size_t)dhalf * 32 * 3072 + doff;
  const unsigned ddst = (unsigned)(uintptr_t)lds + dhalf * 12288 + doff;
  auto stage = [&](int i) {
    const unsigned bo = (i % 3) * K2_STAGE;
#pragma unroll
    for (int j = 0; j < 3; ++j) glds16(dsrc + (size_t)i * 12288 + j * 1024, lane * 16, ddst + bo + j * 1024);
  };
  stage(0);
  stage(1);

  bf16x8 qa0, qa1, qb0, qb1;
  {
    const char* qg_ = ws + WS_QB + ((size_t)head * 64 + qc * 8 + qg * 2) * 1536;
    const bf16x4 z4 = {};
    qa0 = cat44(*(const bf16x4*)(qg_ + h * 512 + r32 * 8), *(const bf16x4*)(qg_ + h * 512 + 256 + r32 * 8));
    qa1 = cat44(*(const bf16x4*)(qg_ + 1024 + r32 * 8), z4);
    qb0 = cat44(*(const bf16x4*)(qg_ + 1536 + h * 512 + r32 * 8), *(const bf16x4*)(qg_ + 1536 + h * 512 + 256 + r32 * 8));
    qb1 = cat44(*(const bf16x4*)(qg_ + 1536 + 1024 + r32 * 8), z4);
    float kn = ((const float*)(ws + WS_KN))[head * 64 + lane];
    kn = rowmax16(kn);
    kn = fmaxf(lanef(kn, 15), lanef(kn, 31)) + fmaxf(lanef(kn, 47), lanef(kn, 63));
    if (h) { qa1 = (bf16x8){}; qb1 = (bf16x8){}; }
    float na = sumsq8(qa0) + sumsq8(qa1), nb = sumsq8(qb0) + sumsq8(qb1);
    na += swap32(na);
    nb += swap32(nb);
    const float ra = sqrtf(na * kn) * 1.01f - 31.f, rb = sqrtf(nb * kn) * 1.01f - 31.f;
    if (!h) {
      qa1[4] = (short)(pack2(-ra, 0.f) & 0xffff);
      qb1[4] = (short)(pack2(-rb, 0.f) & 0xffff);
    }
  }
  const int dv = r32 < 24 ? r32 : (r32 < 28 ? r32 - 4 : r32 - 12);
  const int koff0 = kh * 12288 + h * 512 + r32 * 8, koff1 = kh * 12288 + 1024 + r32 * 8;
#define LDK(p) cat44(*LDS_PTR(const bf16x4, (p)), *LDS_PTR(const bf16x4, (p) + 256))
  const int voff0 = kh * 12288 + 1536 + h * 384 + dv * 16, voff1 = voff0 + 768;

  f32x16 Oa = {}, Ob = {}, Sa, Sb;
  const f32x16 zero = {};
  bf16x8 kf[2][2], vf[2][2];
  WAIT_VM(3);
  RAW_BAR();
  STAMP_AT(1, 1);
  kf[0][0] = LDK(lds + koff0);
  kf[0][1] = LDK(lds + koff1);
  vf[0][0] = *LDS_PTR(const bf16x8, lds + voff0);
  vf[0][1] = *LDS_PTR(const bf16x8, lds + voff1);
  Sa = MFMA32(kf[0][0], qa0, zero);
  Sa = MFMA32(kf[0][1], qa1, Sa);
  u32x4 pk = {};
  u32x4 pc = {};
  float e12 = 0.f, e13 = 0.f, e14 = 0.f, e15 = 0.f;
  bf16x8 vlast = {};
#define EXP2(x) __builtin_amdgcn_exp2f(x)
#define BC8(x) __builtin_bit_cast(bf16x8, x)
#define E_(d, x) d = EXP2(x); PIN(d); SBAR();
#define C_(v, i, x, y) v[i] = pack2(x, y); PIN(v); SBAR();
#define HALF(SC, OC, OP, VP, SN, KN0, KN1, QN0, QN1, V0)                                         \
  do {                                                                                           \
    u32x4 p0;                                                                                    \
    float a0, a1, a2, a3, a4, a5, a6, a7, a8, a9, a10, a11;                                      \
    SBAR();                                                                                      \
    SN = MFMA32(KN0, QN0, zero); PIN(SN); SBAR();                                                \
    E_(a0, SC[0]) C_(pc, 2, e12, e13) E_(a1, SC[1]) C_(pc, 3, e14, e15) E_(a2, SC[2]) E_(a3, SC[3]) \
    OP = MFMA32(VP, BC8(pc), OP); PIN(OP); KEEP(pk); SBAR();                                     \
    E_(a4, SC[4]) E_(a5, SC[5]) C_(p0, 0, a0, a1) E_(a6, SC[6]) E_(a7, SC[7]) C_(p0, 1, a2, a3)  \
    SN = MFMA32(KN1, QN1, SN); PIN(SN); KEEP(pc); SBAR();                                        \
    E_(a8, SC[8]) C_(p0, 2, a4, a5) E_(a9, SC[9]) C_(p0, 3, a6, a7) E_(a10, SC[10]) E_(a11, SC[11]) \
    OC = MFMA32(V0, BC8(p0), OC); PIN(OC); SBAR();                                               \
    E_(e12, SC[12]) C_(pc, 0, a8, a9) E_(e13, SC[13]) C_(pc, 1, a10, a11) E_(e14, SC[14]) E_(e15, SC[15]) \
    pk = p0;                                                                                     \
  } while (0)
#pragma unroll 1
  for (int i = 0; i < 8; ++i) {
    const char* b = lds + (i % 3) * K2_STAGE;
    const char* bn = lds + ((i + 1) % 3) * K2_STAGE;
#pragma unroll
    for (int bb = 0; bb < 4; ++bb) {
      const int c = bb & 1, n = c ^ 1;
      if (bb == 3) {
        WAIT_VM(0);
        if (i == 0) STAMP_AT(1, 5);
        RAW_BAR();
        if (i == 0) STAMP_AT(1, 2);
        if (i + 2 < 8) stage(i + 2);
      }
      if (bb == 3) __builtin_amdgcn_s_setprio(3); else if (bb == 0) __builtin_amdgcn_s_setprio(2);
      else if (bb == 1) __builtin_amdgcn_s_setprio(1); else __builtin_amdgcn_s_setprio(0);
      const char* nx = bb == 3 ? bn : b + (bb + 1) * 3072;
      kf[n][0] = LDK(nx + koff0);
      kf[n][1] = LDK(nx + koff1);
      HALF(Sa, Oa, Ob, vlast, Sb, kf[c][0], kf[c][1], qb0, qb1, vf[c][0]);
      vf[n][0] = *LDS_PTR(const bf16x8, nx + voff0);
      vf[n][1] = *LDS_PTR(const bf16x8, nx + voff1);
      HALF(Sb, Ob, Oa, vf[c][1], Sa, kf[n][0], kf[n][1], qa0, qa1, vf[c][0]);
      vlast = vf[c][1];
    }
  }
  pc[2] = pack2(e12, e13);
  pc[3] = pack2(e14, e15);
  Ob = MFMA32(vlast, BC8(pc), Ob);
  STAMP_AT(1, 3);
  RAW_BAR();
  {
    float* ex = (float*)lds + (size_t)((qg * 2 + kh) * 16) * 64 + lane;
#pragma unroll
    for (int r = 0; r < 16; ++r) *LDS_PTR(float, ex + r * 64) = kh ? Oa[r] : Ob[r];
  }
  __syncthreads();
  f32x16 O;
  {
    const float* ex = (const float*)lds + (size_t)((qg * 2 + (kh ^ 1)) * 16) * 64 + lane;
#pragma unroll
    for (int r = 0; r < 16; ++r) O[r] = (kh ? Ob[r] : Oa[r]) + *LDS_PTR(const float, ex + r * 64);
  }
  float l = h ? O[8] : 0.f;
  l += swap32(l);
  float amax = fabsf(l);
#pragma unroll
  for (int r = 0; r < 12; ++r) amax = fmaxf(amax, fabsf(O[r]));
  const bool bad = !(l >= 8.4703294725430034e-22f  ) || !(amax <= 3.0e38f);
  if (__any(bad)) {
    bf16x8 f0 = kh ? qb0 : qa0, f1 = kh ? qb1 : qa1;
    if (!h) f1[4] = 0;
    attn_fallback(kvh, f0, f1, lane, O);
    l = h ? O[8] : 0.f;
    l += swap32(l);
  }
  const float inv = 1.f / l;
  const int q = (qc * 8 + qg * 2 + kh) * 32 + r32;
  char* og = ws + WS_O + ((size_t)S + q) * (D * 2) + head * 40 + h * 8;
#pragma unroll
  for (int g = 0; g < 3; ++g) {
    if (g < 2 || !h)
      *(u32x2*)(og + g * 16) = (u32x2){pack2(O[4 * g] * inv, O[4 * g + 1] * inv), pack2(O[4 * g + 2] * inv, O[4 * g + 3] * inv)};
  }
  STAMP_AT(1, 4);
}

constexpr int K3_STAGE = 128 * 128 + 64 * 256;
constexpr int K3_LDS = 4 * K3_STAGE;

__global__ __launch_bounds__(768, 3) void k3_proj(const char* __restrict__ ws, const unsigned short* __restrict__ woA,
                                                  const unsigned short* __restrict__ woB, unsigned short* __restrict__ out) {
  extern __shared__ __attribute__((aligned(16))) char lds[];
  const int tid = threadIdx.x, lane = tid & 63;
  const int w = __builtin_amdgcn_readfirstlane(tid >> 6);
  const int xcd = blockIdx.x & 7, slot = blockIdx.x >> 3;
  const int mloc = slot / 5, nblk = slot - mloc * 5;
  const int prob = xcd >> 2, mblk = (xcd & 3) * 4 + mloc;
  const int rot = (2 * nblk + 3 * mloc) % 10;
  const char* X = ws + WS_O + ((size_t)prob * S + mblk * 128) * (D * 2);
  const char* W = (const char*)(prob ? woB : woA) + nblk * 256;
  STAMP_AT(2, 0);
  if (w >= 8) { gemm_loader<128, 4>(X, W, D * 2, rot, lds, w - 8, lane); STAMP_AT(2, 4); return; }
  f32x4 acc[4][2];
  gemm_core<128, 4, false>(lds, w, lane, acc);
  STAMP_AT(2, 2);
  const int wm = w >> 1, wn = w & 1, l15 = lane & 15, kq = lane >> 4;
  char* st = lds + 2 * K3_STAGE + w * 4608;
#pragma unroll
  for (int nb = 0; nb < 4; ++nb) {
#pragma unroll
    for (int mb = 0; mb < 2; ++mb) {
      const f32x4 v = acc[nb][mb];
      *LDS_PTR(u32x2, st + (mb * 16 + l15) * 144 + nb * 32 + kq * 8) = (u32x2){pack2(v[0], v[1]), pack2(v[2], v[3])};
    }
  }
  asm volatile("s_waitcnt lgkmcnt(0)" ::: "memory");
#pragma unroll
  for (int it = 0; it < 4; ++it) {
    const int r = it * 8 + (lane >> 3), c = lane & 7;
    const u32x4 v = *LDS_PTR(const u32x4, st + r * 144 + c * 16);
    ST16(out + (size_t)(mblk * 128 + wm * 32 + r) * (2 * D) + prob * D + nblk * 128 + wn * 64 + c * 8, v);
  }
  STAMP_AT(2, 4);
}

extern "C" void kernel_launch(void* const* d_in, const int* in_sizes, int n_in,
                              void* d_out, int out_size, void* d_ws, size_t ws_size,
                              hipStream_t stream) {
  (void)in_sizes; (void)n_in; (void)out_size; (void)ws_size;
  const unsigned short* xA = (const unsigned short*)d_in[0];
  const unsigned short* wA = (const unsigned short*)d_in[1];
  const unsigned short* oA = (const unsigned short*)d_in[2];
  const unsigned short* xB = (const unsigned short*)d_in[3];
  const unsigned short* wB = (const unsigned short*)d_in[4];
  const unsigned short* oB = (const unsigned short*)d_in[5];
  char* ws = (char*)d_ws;
  (void)hipFuncSetAttribute((const void*)k1_qkv, hipFuncAttributeMaxDynamicSharedMemorySize, K1_LDS);
  (void)hipFuncSetAttribute((const void*)k2_attn, hipFuncAttributeMaxDynamicSharedMemorySize, K2_LDS);
  (void)hipFuncSetAttribute((const void*)k3_proj, hipFuncAttributeMaxDynamicSharedMemorySize, K3_LDS);
  hipLaunchKernelGGL(k1_qkv, dim3(240), dim3(768), K1_LDS, stream, xA, wA, xB, wB, ws);
  hipLaunchKernelGGL(k2_attn, dim3(256), dim3(768), K2_LDS, stream, ws);
  hipLaunchKernelGGL(k3_proj, dim3(160), dim3(768), K3_LDS, stream, (const char*)ws, oA, oB, (unsigned short*)d_out);
}
```

```cpp
#include <hip/hip_runtime.h>
#include <hip/hip_bf16.h>
#include <stdint.h>

typedef __attribute__((ext_vector_type(8))) short bf16x8;
typedef __attribute__((ext_vector_type(4))) short bf16x4;
typedef __attribute__((ext_vector_type(4))) float f32x4;
typedef __attribute__((ext_vector_type(2))) float f32x2;
typedef __attribute__((ext_vector_type(16))) float f32x16;
typedef __attribute__((ext_vector_type(4))) unsigned u32x4;
typedef __attribute__((ext_vector_type(2))) unsigned u32x2;
typedef __attribute__((ext_vector_type(2))) __bf16 bf16x2_t;
typedef __attribute__((address_space(3))) const char* lds_cptr;
typedef short v4i16_t __attribute__((ext_vector_type(4)));

#define DI __device__ __forceinline__
#define LDS_PTR(T, p) ((__attribute__((address_space(3))) T*)(p))

constexpr int S = 2048, D = 640, N3 = 1920, NH = 32, DH = 20;
constexpr float SM_SCALE = 0.22360679774997896f;
constexpr float LOG2E = 1.4426950408889634f;
constexpr float QSCALE = SM_SCALE * LOG2E;

constexpr size_t WS_QB = 0;
constexpr size_t WS_KV = WS_QB + (size_t)NH * 64 * 1536;
constexpr size_t WS_KN = WS_KV + (size_t)NH * 64 * 3072;
constexpr size_t WS_O = WS_KN + 16384;
constexpr size_t WS_AQ = WS_O + (size_t)2 * S * D * 2;
constexpr size_t WS_END = WS_AQ + (size_t)NH * 15 * S * 8 + 4096;

DI unsigned pack2(float lo, float hi) {
  f32x2 v = {lo, hi};
  return __builtin_bit_cast(unsigned, __builtin_convertvector(v, bf16x2_t));
}
DI float rowmax16(float x) {
  x = fmaxf(x, __int_as_float(__builtin_amdgcn_update_dpp(0, __float_as_int(x), 0x111, 0xf, 0xf, true)));
  x = fmaxf(x, __int_as_float(__builtin_amdgcn_update_dpp(0, __float_as_int(x), 0x112, 0xf, 0xf, true)));
  x = fmaxf(x, __int_as_float(__builtin_amdgcn_update_dpp(0, __float_as_int(x), 0x114, 0xf, 0xf, true)));
  x = fmaxf(x, __int_as_float(__builtin_amdgcn_update_dpp(0, __float_as_int(x), 0x118, 0xf, 0xf, true)));
  return x;
}
DI float lanef(float x, int l) { return __int_as_float(__builtin_amdgcn_readlane(__float_as_int(x), l)); }
DI float dot2(unsigned a, unsigned b, float c) {
  return __builtin_amdgcn_fdot2_f32_bf16(__builtin_bit_cast(bf16x2_t, a), __builtin_bit_cast(bf16x2_t, b), c, false);
}
DI float bf_lo(unsigned u) { return __uint_as_float(u << 16); }
DI float bf_hi(unsigned u) { return __uint_as_float(u & 0xffff0000u); }
DI void glds16(const char* base, unsigned voff, unsigned lds_a) {
  unsigned keep;
  const uint64_t b64 = (uint64_t)base;
  const uint64_t sbase = ((uint64_t)(unsigned)__builtin_amdgcn_readfirstlane((unsigned)(b64 >> 32)) << 32) |
                         (unsigned)__builtin_amdgcn_readfirstlane((unsigned)b64);
  const unsigned lds_addr = __builtin_amdgcn_readfirstlane(lds_a);
  asm volatile("s_mov_b32 %0, m0\n\ts_mov_b32 m0, %3\n\ts_nop 0\n\tglobal_load_lds_dwordx4 %1, %2\n\ts_mov_b32 m0, %0"
               : "=&s"(keep) : "v"(voff), "s"(sbase), "s"(lds_addr) : "memory");
}
#define SBAR() __builtin_amdgcn_sched_barrier(0)
#define PIN(x) asm volatile("" : "+v"(x))
#define KEEP(x) asm volatile("" :: "v"(x))
DI bf16x4 tr_read(const char* p) {
  return __builtin_bit_cast(bf16x4, __builtin_amdgcn_ds_read_tr16_b64_v4i16(LDS_PTR(v4i16_t, p)));
}
#define ST16(p, v) __builtin_nontemporal_store((u32x4)(v), (u32x4*)(p))
#define WAIT_VM(n) asm volatile("s_waitcnt vmcnt(" #n ")" ::: "memory")
#define WAIT_LGKM0() __builtin_amdgcn_s_waitcnt(0xC07F)
#define RAW_BAR() do { asm volatile("" ::: "memory"); WAIT_LGKM0(); __builtin_amdgcn_s_barrier(); asm volatile("" ::: "memory"); } while (0)

template <int BM, int NBUF>
DI void gemm_loader(const char* X, const char* W, unsigned ldw, int rot, char* lds, int w, int lane) {
  constexpr int XB = BM * 128, STAGE = XB + 64 * 256, NX = BM / 32, NP = NX + 4;
  __builtin_amdgcn_s_setprio(3);
  unsigned src[NP], dst[NP];
  const unsigned lds0 = (unsigned)(uintptr_t)lds;
#pragma unroll
  for (int j = 0; j < NX; ++j) {
    const int i = w + 4 * j, row = i * 8 + (lane >> 3), c = (lane & 7) ^ (row & 7);
    src[j] = row * (D * 2) + c * 16;
    dst[j] = lds0 + i * 1024;
  }
#pragma unroll
  for (int j = 0; j < 4; ++j) {
    const int i = w + 4 * j, rho = i * 4 + (lane >> 4), c16 = lane & 15;
    const int k = (rho & ~12) | ((rho & 4) << 1) | ((rho & 8) >> 1);
    src[NX + j] = k * ldw + (((c16 >> 1) ^ (rho & 7)) << 5) + (c16 & 1) * 16;
    dst[NX + j] = lds0 + XB + i * 1024;
  }
  auto stage = [&](int kt, int buf) {
    int k = kt + rot; if (k >= 10) k -= 10;
    const char* xs = X + k * 128;
    const char* wsrc = W + (size_t)k * 64 * ldw;
#pragma unroll
    for (int j = 0; j < 4; ++j) glds16(wsrc, src[NX + j], dst[NX + j] + buf * STAGE);
#pragma unroll
    for (int j = 0; j < NX; ++j) glds16(xs, src[j], dst[j] + buf * STAGE);
  };
  auto wait_stages = [&](int n) {
    if (n <= 0) WAIT_VM(0);
    else if (n == 1) { if (NP == 12) WAIT_VM(12); else WAIT_VM(8); }
    else if (n == 2) { if (NP == 12) WAIT_VM(24); else WAIT_VM(16); }
    else if (n == 3) { if (NP == 12) WAIT_VM(36); else WAIT_VM(24); }
    else WAIT_VM(32);
  };
#pragma unroll
  for (int i = 0; i < NBUF; ++i) stage(i, i);
  wait_stages(NBUF - 1);
  __builtin_amdgcn_s_barrier();
  int buf = 0;
#pragma unroll 1
  for (int kt = 0; kt < 10; ++kt) {
    wait_stages((kt + NBUF - 1 < 9 ? kt + NBUF - 1 : 9) - (kt + 1));
    __builtin_amdgcn_s_barrier();
    if (kt + NBUF < 10) stage(kt + NBUF, buf);
    buf = buf == NBUF - 1 ? 0 : buf + 1;
  }
}

template <int BM, int NBUF, bool SWAP>
DI void gemm_core(char* lds, int w, int lane, f32x4 (&acc)[4][BM / 64]) {
  constexpr int MB = BM / 64, XB = BM * 128, STAGE = XB + 64 * 256;
  const int wm = w >> 1, wn = w & 1, l15 = lane & 15, kq = lane >> 4;
  int xoff[MB][2], woff[4];
#pragma unroll
  for (int mb = 0; mb < MB; ++mb) {
    const int row = wm * (BM / 4) + mb * 16 + l15;
#pragma unroll
    for (int ks = 0; ks < 2; ++ks) xoff[mb][ks] = row * 128 + (((ks * 4 + kq) ^ (row & 7)) << 4);
  }
  {
    const int r7 = (kq & 1) * 4 + (l15 >> 2);
#pragma unroll
    for (int nb = 0; nb < 4; ++nb) woff[nb] = XB + ((kq >> 1) * 16 + r7) * 256 + (((wn * 4 + nb) ^ r7) << 5) + (l15 & 3) * 8;
  }
#pragma unroll
  for (int a = 0; a < 4; ++a)
#pragma unroll
    for (int b = 0; b < MB; ++b) acc[a][b] = (f32x4){0.f, 0.f, 0.f, 0.f};
  bf16x8 xf[2][MB], wf[2][4];
  auto rd = [&](const char* b, int ks) {
#pragma unroll
    for (int mb = 0; mb < MB; ++mb) xf[ks][mb] = *LDS_PTR(const bf16x8, b + xoff[mb][ks]);
#pragma unroll
    for (int nb = 0; nb < 4; ++nb) {
      const bf16x4 lo = tr_read(b + woff[nb] + ks * (32 * 256));
      const bf16x4 hi = tr_read(b + woff[nb] + ks * (32 * 256) + 8 * 256);
      wf[ks][nb] = __builtin_shufflevector(lo, hi, 0, 1, 2, 3, 4, 5, 6, 7);
    }
  };
  auto mm = [&](int ks) {
#pragma unroll
    for (int nb = 0; nb < 4; ++nb)
#pragma unroll
      for (int mb = 0; mb < MB; ++mb)
        acc[nb][mb] = SWAP ? __builtin_amdgcn_mfma_f32_16x16x32_bf16(xf[ks][mb], wf[ks][nb], acc[nb][mb], 0, 0, 0)
                           : __builtin_amdgcn_mfma_f32_16x16x32_bf16(wf[ks][nb], xf[ks][mb], acc[nb][mb], 0, 0, 0);
  };
  auto interleave = [&]() {
#pragma unroll
    for (int i = 0; i < 4 * MB; ++i) {
      __builtin_amdgcn_sched_group_barrier(0x008, 1, 0);
      __builtin_amdgcn_sched_group_barrier(0x100, MB == 4 ? 1 : 2, 0);
    }
  };
  RAW_BAR();
  rd(lds, 0);
  int buf = 0;
#pragma unroll 1
  for (int kt = 0; kt < 10; ++kt) {
    const char* b = lds + buf * STAGE;
    buf = buf == NBUF - 1 ? 0 : buf + 1;
    WAIT_LGKM0();
    SBAR();
    rd(b, 1);
    mm(0);
    interleave();
    SBAR();
    RAW_BAR();
    rd(lds + buf * STAGE, 0);
    mm(1);
    interleave();
    SBAR();
  }
  WAIT_LGKM0();
  asm volatile("" ::: "memory");
}

constexpr int K1_LDS = 3 * (256 * 128 + 64 * 256);

__global__ __launch_bounds__(768, 3) void k1_qkv(const unsigned short* __restrict__ xA, const unsigned short* __restrict__ wA,
                                                 const unsigned short* __restrict__ xB, const unsigned short* __restrict__ wB,
                                                 char* __restrict__ ws) {
  extern __shared__ __attribute__((aligned(16))) char lds[];
  const int tid = threadIdx.x, lane = tid & 63;
  const int w = __builtin_amdgcn_readfirstlane(tid >> 6);
  const int xcd = blockIdx.x & 7, slot = blockIdx.x >> 3;
  const int mloc = slot / 15, nblk = slot - mloc * 15;
  const int prob = xcd >> 2, mblk = (xcd & 3) * 2 + mloc;
  const int rot = (nblk + 5 * mloc) % 10;
  const char* X = (const char*)(prob ? xB : xA) + (size_t)mblk * 256 * (D * 2);
  const char* W = (const char*)(prob ? wB : wA) + nblk * 256;
  const int seg = nblk / 5;
  const bool swap = prob && seg == 2;

  if (w >= 8) { gemm_loader<256, 3>(X, W, N3 * 2, rot, lds, w - 8, lane); return; }
  f32x4 acc[4][4];
  if (swap) gemm_core<256, 3, true>(lds, w, lane, acc);
  else gemm_core<256, 3, false>(lds, w, lane, acc);

  const int wm = w >> 1, wn = w & 1, l15 = lane & 15, kq = lane >> 4;
  const int c0 = (nblk - seg * 5) * 128 + wn * 64;
  const int row0 = mblk * 256 + wm * 64;
  char* st = lds + (w < 6 ? 2 * 49152 + w * 8192 : (w - 6) * 8192);
  if (!swap) {
    const float sc = (prob && seg == 0) ? QSCALE : 1.f;
#pragma unroll
    for (int nb = 0; nb < 4; ++nb)
#pragma unroll
      for (int mb = 0; mb < 4; ++mb) {
        const f32x4 v = acc[nb][mb] * sc;
        *LDS_PTR(u32x2, st + (nb * 4 + kq) * 512 + (mb * 16 + l15) * 8) = (u32x2){pack2(v[0], v[1]), pack2(v[2], v[3])};
      }
    WAIT_LGKM0();
    asm volatile("" ::: "memory");
    if (prob && seg == 1) {
      float* kn = (float*)(ws + WS_KN) + (row0 >> 6);
      u32x2 qv[16];
#pragma unroll
      for (int q = 0; q < 16; ++q) qv[q] = *LDS_PTR(const u32x2, st + q * 512 + lane * 8);
      float s = 0.f;
      int hcur = c0 / 20;
#pragma unroll
      for (int q = 0; q < 16; ++q) {
        const unsigned u0 = qv[q][0], u1 = qv[q][1];
        float x;
        x = bf_lo(u0); s = fmaf(x, x, s); x = bf_hi(u0); s = fmaf(x, x, s);
        x = bf_lo(u1); s = fmaf(x, x, s); x = bf_hi(u1); s = fmaf(x, x, s);
        const bool hend = c0 + q * 4 + 4 == (hcur + 1) * 20;
        if (hend || q == 15) {
          s = rowmax16(s);
          s = fmaxf(fmaxf(lanef(s, 15), lanef(s, 31)), fmaxf(lanef(s, 47), lanef(s, 63)));
          const bool first = hcur * 20 >= c0;
          if (lane == 0) {
            kn[(hcur * 2 + (first ? 0 : 1)) * 32] = s;
            if (first && hend) kn[(hcur * 2 + 1) * 32] = 0.f;
          }
          s = 0.f; ++hcur;
        }
      }
    }
    const int tp = lane & 31;
#pragma unroll
    for (int it = 0; it < 8; ++it) {
      const u32x4 v = *LDS_PTR(const u32x4, st + it * 1024 + lane * 16);
      const int q = (c0 >> 2) + it * 2 + (lane >> 5), hh = q / 5, dq = q - hh * 5;
      if (!prob) {
        ST16(ws + WS_AQ + ((size_t)(hh * 15 + seg * 5 + dq) * S + row0 + 2 * tp) * 8, v);
      } else {
        char* g = ws + (seg ? WS_KV : WS_QB) + ((size_t)hh * 64 + (row0 >> 5) + (tp >> 4)) * (seg ? 3072 : 1536) + dq * 256 + (tp & 15) * 16;
        ST16(g, v);
        if (seg && dq == 4) ST16(g + 256, ((u32x4){0x3F80u, 0u, 0x3F80u, 0u}));
      }
    }
  } else {
#pragma unroll
    for (int nb = 0; nb < 4; ++nb)
#pragma unroll
      for (int mb = 0; mb < 4; ++mb) {
        const f32x4 v = acc[nb][mb];
        *LDS_PTR(u32x2, st + (mb * 2 + (kq & 1)) * 1024 + (nb * 16 + l15) * 16 + (kq >> 1) * 8) = (u32x2){pack2(v[0], v[1]), pack2(v[2], v[3])};
      }
    WAIT_LGKM0();
    asm volatile("" ::: "memory");
    const int c = c0 + lane, hh = c / 20, d = c - hh * 20;
    char* g = ws + WS_KV + ((size_t)hh * 64 + (row0 >> 5)) * 3072 + 1536 + d * 16;
#pragma unroll
    for (int it = 0; it < 8; ++it) {
      const u32x4 v = *LDS_PTR(const u32x4, st + it * 1024 + lane * 16);
      ST16(g + (it >> 2) * 3072 + (it & 3) * 384, v);
    }
#pragma unroll
    for (int it = 0; it < 2; ++it) {
      const int h2 = (c0 + 19) / 20 + it * 2 + (lane >> 5), blk = (lane >> 4) & 1, sh = (lane >> 2) & 3, d2 = 20 + (lane & 3);
      const unsigned o = d2 == 20 ? 0x3F803F80u : 0u;
      if (h2 * 20 < c0 + 64)
        ST16(ws + WS_KV + ((size_t)h2 * 64 + (row0 >> 5) + blk) * 3072 + 1536 + sh * 384 + d2 * 16, ((u32x4){o, o, o, o}));
    }
  }

}

constexpr int K2_HALFB = 8 * 3072;
constexpr int K2_STAGE = 2 * K2_HALFB;
constexpr int K2_LOC = 15 * 640;
constexpr int K2_LDS = 3 * K2_STAGE;
#define MFMA32(a, b, c) __builtin_amdgcn_mfma_f32_32x32x16_bf16(a, b, c, 0, 0, 0)

DI bf16x8 cat44(bf16x4 a, bf16x4 b) { return __builtin_shufflevector(a, b, 0, 1, 2, 3, 4, 5, 6, 7); }
DI void exp_pack(const f32x16& s, bf16x8& p0, bf16x8& p1) {
  u32x4 a, b;
#pragma unroll
  for (int i = 0; i < 4; ++i) {
    a[i] = pack2(__builtin_amdgcn_exp2f(s[2 * i]), __builtin_amdgcn_exp2f(s[2 * i + 1]));
    b[i] = pack2(__builtin_amdgcn_exp2f(s[8 + 2 * i]), __builtin_amdgcn_exp2f(s[8 + 2 * i + 1]));
  }
  p0 = __builtin_bit_cast(bf16x8, a);
  p1 = __builtin_bit_cast(bf16x8, b);
}
DI float swap32(float x) {
  const auto r = __builtin_amdgcn_permlane32_swap(__float_as_uint(x), __float_as_uint(x), false, false);
  return __uint_as_float((threadIdx.x & 32) ? r[0] : r[1]);
}
DI float sumsq8(bf16x8 f) {
  const u32x4 u = __builtin_bit_cast(u32x4, f);
  float s = 0.f;
#pragma unroll
  for (int i = 0; i < 4; ++i) { float x = bf_lo(u[i]); s = fmaf(x, x, s); x = bf_hi(u[i]); s = fmaf(x, x, s); }
  return s;
}

DI void attn_fallback(const char* kvh, bf16x8 q0, bf16x8 q1, int lane, f32x16& O) {
  const int r32 = lane & 31, h = lane >> 5;
  const int dv = r32 < 24 ? r32 : (r32 < 28 ? r32 - 4 : r32 - 12);
  float m = -INFINITY;
  O = (f32x16){};
#pragma unroll 1
  for (int t = 0; t < 64; ++t) {
    const char* g = kvh + (size_t)t * 3072;
    const bf16x8 k0 = cat44(*(const bf16x4*)(g + h * 512 + r32 * 8), *(const bf16x4*)(g + h * 512 + 256 + r32 * 8));
    const bf16x8 k1 = cat44(*(const bf16x4*)(g + 1024 + r32 * 8), *(const bf16x4*)(g + 1280 + r32 * 8));
    const bf16x8 v0 = *(const bf16x8*)(g + 1536 + h * 384 + dv * 16);
    const bf16x8 v1 = *(const bf16x8*)(g + 1536 + 768 + h * 384 + dv * 16);
    f32x16 s = MFMA32(k0, q0, (f32x16){});
    s = MFMA32(k1, q1, s);
    float pm = s[0];
#pragma unroll
    for (int i = 1; i < 16; ++i) pm = fmaxf(pm, s[i]);
    pm = fmaxf(pm, swap32(pm));
    const float mn = fmaxf(m, pm);
    const float alpha = __builtin_amdgcn_exp2f(m - mn);
    m = mn;
#pragma unroll
    for (int i = 0; i < 16; ++i) { s[i] -= mn; O[i] *= alpha; }
    bf16x8 p0, p1;
    exp_pack(s, p0, p1);
    O = MFMA32(v0, p0, O);
    O = MFMA32(v1, p1, O);
  }
}

template <bool MASK>
DI void local_attn(const char* me, int s, float (&o)[20]) {
  u32x2 q[5], kv[8][5];
#pragma unroll
  for (int i = 0; i < 5; ++i) q[i] = *LDS_PTR(const u32x2, me + i * 640);
#pragma unroll
  for (int j = 0; j < 8; ++j) {
    const char* kr = me - (MASK ? min(2 * j, s) : 2 * j) * 8 + 5 * 640;
#pragma unroll
    for (int i = 0; i < 5; ++i) kv[j][i] = *LDS_PTR(const u32x2, kr + i * 640);
  }
  SBAR();
  float lg[8];
  float mx = -INFINITY;
#pragma unroll
  for (int j = 0; j < 8; ++j) {
    float a0 = 0.f, a1 = 0.f;
#pragma unroll
    for (int i = 0; i < 5; ++i) {
      const unsigned x0 = kv[j][i][0], x1 = kv[j][i][1], y0 = q[i][0], y1 = q[i][1];
      a0 = dot2(x0, y0, a0);
      a1 = dot2(x1, y1, a1);
    }
    lg[j] = (a0 + a1) * QSCALE;
    if (MASK && s < 2 * j) lg[j] = -INFINITY;
    mx = fmaxf(mx, lg[j]);
  }
  SBAR();
#pragma unroll
  for (int j = 0; j < 8; ++j) {
    const char* vr = me - (MASK ? min(2 * j, s) : 2 * j) * 8 + 10 * 640;
#pragma unroll
    for (int i = 0; i < 5; ++i) kv[j][i] = *LDS_PTR(const u32x2, vr + i * 640);
  }
  SBAR();
  float sum = 0.f;
#pragma unroll
  for (int j = 0; j < 8; ++j) { lg[j] = __builtin_amdgcn_exp2f(lg[j] - mx); sum += lg[j]; }
  const float inv = 1.f / sum;
#pragma unroll
  for (int i = 0; i < 20; ++i) o[i] = 0.f;
#pragma unroll
  for (int j = 0; j < 8; ++j) {
    const unsigned pl = pack2(lg[j] * inv, 0.f), ph = pl << 16;
#pragma unroll
    for (int i = 0; i < 5; ++i) {
      const unsigned x0 = kv[j][i][0], x1 = kv[j][i][1];
      o[4 * i + 0] = dot2(x0, pl, o[4 * i + 0]);
      o[4 * i + 1] = dot2(x0, ph, o[4 * i + 1]);
      o[4 * i + 2] = dot2(x1, pl, o[4 * i + 2]);
      o[4 * i + 3] = dot2(x1, ph, o[4 * i + 3]);
    }
  }
}

__global__ __launch_bounds__(768, 3) void k2_attn(char* __restrict__ ws, const char* __restrict__ woA, const char* __restrict__ woB) {
  extern __shared__ __attribute__((aligned(16))) char lds[];
  const int tid = threadIdx.x, lane = tid & 63;
  const int w = __builtin_amdgcn_readfirstlane(tid >> 6);


  if (w >= 8) {
    __builtin_amdgcn_s_setprio(3);
    const int task = blockIdx.x * 4 + (w - 8), hA = task >> 5, r0 = (task & 31) * 64;
    char* L = lds + 2 * K2_STAGE + (w - 8) * K2_LOC;
    const char* g = ws + WS_AQ + ((ptrdiff_t)hA * 15 * S + r0 - 16) * 8;
    __builtin_amdgcn_s_barrier();
    if (lane < 40) {
#pragma unroll
      for (int i = 0; i < 15; ++i) glds16(g + (size_t)i * S * 8, lane * 16, (unsigned)(uintptr_t)L + i * 640);
    }
    WAIT_VM(0);

    const unsigned line = min(((blockIdx.x >> 3) * 4 + (w - 8)) * 64 + lane, 6399u);
    const unsigned pf = *(const unsigned*)(((blockIdx.x & 4) ? woB : woA) + (size_t)line * 128);
    SBAR();
    const int s = r0 + lane;
    float o[20];
    if (r0 == 0) local_attn<true>(L + (lane + 16) * 8, s, o); else local_attn<false>(L + (lane + 16) * 8, s, o);
    char* og = ws + WS_O + (size_t)s * (D * 2) + hA * 40;
#pragma unroll
    for (int i = 0; i < 5; ++i) *(u32x2*)(og + i * 8) = (u32x2){pack2(o[4 * i], o[4 * i + 1]), pack2(o[4 * i + 2], o[4 * i + 3])};
    asm volatile("" :: "v"(pf));

    return;
  }

  const int xcd = blockIdx.x & 7, slot = blockIdx.x >> 3;
  const int head = xcd * 4 + (slot >> 3), qc = slot & 7;
  const int qg = w & 3, kh = w >> 2;
  const int r32 = lane & 31, h = lane >> 5;
  const char* kvh = ws + WS_KV + (size_t)head * 64 * 3072;

  const int dhalf = w >> 2, doff = (w & 3) * 6144;
  const char* dsrc = kvh + (size_t)dhalf * 32 * 3072 + doff;
  const unsigned ddst = (unsigned)(uintptr_t)lds + dhalf * K2_HALFB + doff;
  auto stage = [&](int i) {
    const unsigned bo = (i == 3 ? 0 : i) * K2_STAGE;
#pragma unroll
    for (int j = 0; j < 6; ++j) glds16(dsrc + (size_t)i * K2_HALFB + j * 1024, lane * 16, ddst + bo + j * 1024);
  };
  stage(0);
  stage(1);

  bf16x8 qa0, qa1, qb0, qb1;
  {
    const char* qg_ = ws + WS_QB + ((size_t)head * 64 + qc * 8 + qg * 2) * 1536;
    const bf16x4 z4 = {};
    qa0 = cat44(*(const bf16x4*)(qg_ + h * 512 + r32 * 8), *(const bf16x4*)(qg_ + h * 512 + 256 + r32 * 8));
    qa1 = cat44(*(const bf16x4*)(qg_ + 1024 + r32 * 8), z4);
    qb0 = cat44(*(const bf16x4*)(qg_ + 1536 + h * 512 + r32 * 8), *(const bf16x4*)(qg_ + 1536 + h * 512 + 256 + r32 * 8));
    qb1 = cat44(*(const bf16x4*)(qg_ + 1536 + 1024 + r32 * 8), z4);
    float kn = rowmax16(((const float*)(ws + WS_KN))[head * 64 + lane]);
    kn = fmaxf(lanef(kn, 15), lanef(kn, 31)) + fmaxf(lanef(kn, 47), lanef(kn, 63));
    if (h) { qa1 = (bf16x8){}; qb1 = (bf16x8){}; }
    float na = sumsq8(qa0) + sumsq8(qa1), nb = sumsq8(qb0) + sumsq8(qb1);
    na += swap32(na);
    nb += swap32(nb);
    const float ra = sqrtf(na * kn) * 1.01f - 31.f, rb = sqrtf(nb * kn) * 1.01f - 31.f;
    if (!h) {
      qa1[4] = (short)(pack2(-ra, 0.f) & 0xffff);
      qb1[4] = (short)(pack2(-rb, 0.f) & 0xffff);
    }
  }
  const int dv = min(r32, 21);
  const int koff0 = kh * K2_HALFB + h * 512 + r32 * 8, koff1 = kh * K2_HALFB + 1024 + r32 * 8;
#define LDK(p) cat44(*LDS_PTR(const bf16x4, (p)), *LDS_PTR(const bf16x4, (p) + 256))
  const int voff0 = kh * K2_HALFB + 1536 + h * 384 + dv * 16, voff1 = voff0 + 768;

  f32x16 Oa = {}, Ob = {}, Sa, Sb;
  const f32x16 zero = {};
  bf16x8 kf[2][2], vf[2][2];
  WAIT_VM(6);
  RAW_BAR();

  __builtin_amdgcn_s_setprio(3);
  kf[0][0] = LDK(lds + koff0);
  kf[0][1] = LDK(lds + koff1);
  vf[0][0] = *LDS_PTR(const bf16x8, lds + voff0);
  vf[0][1] = *LDS_PTR(const bf16x8, lds + voff1);
  Sa = MFMA32(kf[0][0], qa0, zero);
  Sa = MFMA32(kf[0][1], qa1, Sa);
  u32x4 pk = {};
  u32x4 pc = {};
  float e12 = 0.f, e13 = 0.f, e14 = 0.f, e15 = 0.f;
  bf16x8 vlast = {};
#define EXP2(x) __builtin_amdgcn_exp2f(x)
#define BC8(x) __builtin_bit_cast(bf16x8, x)
#define E_(d, x) d = EXP2(x); PIN(d); SBAR();
#define C_(v, i, x, y) v[i] = pack2(x, y); PIN(v); SBAR();
#define HALF(SC, OC, OP, VP, SN, KN0, KN1, QN0, QN1, V0)                                         \
  do {                                                                                           \
    u32x4 p0;                                                                                    \
    float a0, a1, a2, a3, a4, a5, a6, a7, a8, a9, a10, a11;                                      \
    SBAR();                                                                                      \
    SN = MFMA32(KN0, QN0, zero); PIN(SN); SBAR();                                                \
    E_(a0, SC[0]) C_(pc, 2, e12, e13) E_(a1, SC[1]) C_(pc, 3, e14, e15) E_(a2, SC[2]) E_(a3, SC[3]) \
    OP = MFMA32(VP, BC8(pc), OP); PIN(OP); KEEP(pk); SBAR();                                     \
    E_(a4, SC[4]) E_(a5, SC[5]) C_(p0, 0, a0, a1) E_(a6, SC[6]) E_(a7, SC[7]) C_(p0, 1, a2, a3)  \
    SN = MFMA32(KN1, QN1, SN); PIN(SN); KEEP(pc); SBAR();                                        \
    E_(a8, SC[8]) C_(p0, 2, a4, a5) E_(a9, SC[9]) C_(p0, 3, a6, a7) E_(a10, SC[10]) E_(a11, SC[11]) \
    OC = MFMA32(V0, BC8(p0), OC); PIN(OC); SBAR();                                               \
    E_(e12, SC[12]) C_(pc, 0, a8, a9) E_(e13, SC[13]) C_(pc, 1, a10, a11) E_(e14, SC[14]) E_(e15, SC[15]) \
    pk = p0;                                                                                     \
  } while (0)
#pragma unroll 1
  for (int i = 0; i < 8; ++i) {
    const int sg = i >> 1;
    const char* b = lds + (sg == 3 ? 0 : sg) * K2_STAGE + (i & 1) * (4 * 3072);
    const char* bn = (i & 1) ? lds + (sg >= 2 ? 0 : sg + 1) * K2_STAGE : b + 4 * 3072;
#pragma unroll
    for (int bb = 0; bb < 4; ++bb) {
      const int c = bb & 1, n = c ^ 1;
      if (bb == 3 && (i & 1)) {
        WAIT_VM(0);
        RAW_BAR();
        if (sg + 2 < 4) stage(sg + 2);
      }
      if (bb == 3) { if (i & 1) __builtin_amdgcn_s_setprio(3); else __builtin_amdgcn_s_setprio(1); }
      else if (bb == 1) { if (i & 1) __builtin_amdgcn_s_setprio(0); else __builtin_amdgcn_s_setprio(2); }
      const char* nx = bb == 3 ? bn : b + (bb + 1) * 3072;
      kf[n][0] = LDK(nx + koff0);
      kf[n][1] = LDK(nx + koff1);
      HALF(Sa, Oa, Ob, vlast, Sb, kf[c][0], kf[c][1], qb0, qb1, vf[c][0]);
      vf[n][0] = *LDS_PTR(const bf16x8, nx + voff0);
      vf[n][1] = *LDS_PTR(const bf16x8, nx + voff1);
      HALF(Sb, Ob, Oa, vf[c][1], Sa, kf[n][0], kf[n][1], qa0, qa1, vf[c][0]);
      vlast = vf[c][1];
    }
  }
  pc[2] = pack2(e12, e13);
  pc[3] = pack2(e14, e15);
  Ob = MFMA32(vlast, BC8(pc), Ob);

  RAW_BAR();
  {
    float* ex = (float*)lds + (size_t)((qg * 2 + kh) * 16) * 64 + lane;
#pragma unroll
    for (int r = 0; r < 16; ++r) *LDS_PTR(float, ex + r * 64) = kh ? Oa[r] : Ob[r];
  }
  __syncthreads();
  f32x16 O;
  {
    const float* ex = (const float*)lds + (size_t)((qg * 2 + (kh ^ 1)) * 16) * 64 + lane;
#pragma unroll
    for (int r = 0; r < 16; ++r) O[r] = (kh ? Ob[r] : Oa[r]) + *LDS_PTR(const float, ex + r * 64);
  }
  float l = h ? O[8] : 0.f;
  l += swap32(l);
  float amax = fabsf(l);
#pragma unroll
  for (int r = 0; r < 12; ++r) amax = fmaxf(amax, fabsf(O[r]));
  const bool bad = !(l >= 8.4703294725430034e-22f  ) || !(amax <= 3.0e38f);
  if (__any(bad)) {
    bf16x8 f0 = kh ? qb0 : qa0, f1 = kh ? qb1 : qa1;
    if (!h) f1[4] = 0;
    attn_fallback(kvh, f0, f1, lane, O);
    l = h ? O[8] : 0.f;
    l += swap32(l);
  }
  const float inv = 1.f / l;
  const int q = (qc * 8 + qg * 2 + kh) * 32 + r32;
  char* og = ws + WS_O + ((size_t)S + q) * (D * 2) + head * 40 + h * 8;
#pragma unroll
  for (int g = 0; g < 3; ++g) {
    if (g < 2 || !h)
      *(u32x2*)(og + g * 16) = (u32x2){pack2(O[4 * g] * inv, O[4 * g + 1] * inv), pack2(O[4 * g + 2] * inv, O[4 * g + 3] * inv)};
  }

}

constexpr int K3_LDS = 5 * (128 * 128 + 64 * 256);

__global__ __launch_bounds__(768, 3) void k3_proj(const char* __restrict__ ws, const unsigned short* __restrict__ woA,
                                                  const unsigned short* __restrict__ woB, unsigned short* __restrict__ out) {
  extern __shared__ __attribute__((aligned(16))) char lds[];
  const int tid = threadIdx.x, lane = tid & 63;
  const int w = __builtin_amdgcn_readfirstlane(tid >> 6);
  const int xcd = blockIdx.x & 7, slot = blockIdx.x >> 3;
  const int mloc = slot / 5, nblk = slot - mloc * 5;
  const int prob = xcd >> 2, mblk = (xcd & 3) * 4 + mloc;
  const int rot = (2 * nblk + 3 * mloc) % 10;
  const char* X = ws + WS_O + ((size_t)prob * S + mblk * 128) * (D * 2);
  const char* W = (const char*)(prob ? woB : woA) + nblk * 256;

  if (w >= 8) { gemm_loader<128, 5>(X, W, D * 2, rot, lds, w - 8, lane); return; }
  f32x4 acc[4][2];
  gemm_core<128, 5, false>(lds, w, lane, acc);
  const int wm = w >> 1, wn = w & 1, l15 = lane & 15, kq = lane >> 4;
  char* st = lds + 2 * 32768 + w * 4096;
#pragma unroll
  for (int nb = 0; nb < 4; ++nb)
#pragma unroll
    for (int mb = 0; mb < 2; ++mb) {
      const f32x4 v = acc[nb][mb];
      const int r = mb * 16 + l15;
      *LDS_PTR(u32x2, st + r * 128 + (((nb * 2 + (kq >> 1)) ^ (r & 7)) << 4) + (kq & 1) * 8) = (u32x2){pack2(v[0], v[1]), pack2(v[2], v[3])};
    }
  WAIT_LGKM0();
  asm volatile("" ::: "memory");
#pragma unroll
  for (int it = 0; it < 4; ++it) {
    const int r = it * 8 + (lane >> 3), c = lane & 7;
    const u32x4 v = *LDS_PTR(const u32x4, st + r * 128 + ((c ^ (r & 7)) << 4));
    ST16(out + (size_t)(mblk * 128 + wm * 32 + r) * (2 * D) + prob * D + nblk * 128 + wn * 64 + c * 8, v);
  }

}

extern "C" void kernel_launch(void* const* d_in, const int* in_sizes, int n_in,
                              void* d_out, int out_size, void* d_ws, size_t ws_size,
                              hipStream_t stream) {
  (void)in_sizes; (void)n_in; (void)out_size; (void)ws_size;
  const unsigned short* xA = (const unsigned short*)d_in[0];
  const unsigned short* wA = (const unsigned short*)d_in[1];
  const unsigned short* oA = (const unsigned short*)d_in[2];
  const unsigned short* xB = (const unsigned short*)d_in[3];
  const unsigned short* wB = (const unsigned short*)d_in[4];
  const unsigned short* oB = (const unsigned short*)d_in[5];
  char* ws = (char*)d_ws;
  (void)hipFuncSetAttribute((const void*)k1_qkv, hipFuncAttributeMaxDynamicSharedMemorySize, K1_LDS);
  (void)hipFuncSetAttribute((const void*)k2_attn, hipFuncAttributeMaxDynamicSharedMemorySize, K2_LDS);
  (void)hipFuncSetAttribute((const void*)k3_proj, hipFuncAttributeMaxDynamicSharedMemorySize, K3_LDS);
  hipLaunchKernelGGL(k1_qkv, dim3(240), dim3(768), K1_LDS, stream, xA, wA, xB, wB, ws);
  hipLaunchKernelGGL(k2_attn, dim3(256), dim3(768), K2_LDS, stream, ws, (const char*)oA, (const char*)oB);
  hipLaunchKernelGGL(k3_proj, dim3(160), dim3(768), K3_LDS, stream, (const char*)ws, oA, oB, (unsigned short*)d_out);
}
```

```cpp
#include <hip/hip_runtime.h>
#include <hip/hip_bf16.h>
#include <stdint.h>

typedef __attribute__((ext_vector_type(8))) short bf16x8;
typedef __attribute__((ext_vector_type(4))) short bf16x4;
typedef __attribute__((ext_vector_type(4))) float f32x4;
typedef __attribute__((ext_vector_type(2))) float f32x2;
typedef __attribute__((ext_vector_type(16))) float f32x16;
typedef __attribute__((ext_vector_type(4))) unsigned u32x4;
typedef __attribute__((ext_vector_type(2))) unsigned u32x2;
typedef __attribute__((ext_vector_type(2))) __bf16 bf16x2_t;
typedef __attribute__((address_space(3))) const char* lds_cptr;
typedef short v4i16_t __attribute__((ext_vector_type(4)));

#define DI __device__ __forceinline__
#define LDS_PTR(T, p) ((__attribute__((address_space(3))) T*)(p))

constexpr int S = 2048, D = 640, N3 = 1920, NH = 32, DH = 20;
constexpr float SM_SCALE = 0.22360679774997896f;
constexpr float LOG2E = 1.4426950408889634f;
constexpr float QSCALE = SM_SCALE * LOG2E;

constexpr size_t WS_QB = 0;
constexpr size_t WS_KV = WS_QB + (size_t)NH * 64 * 1536;
constexpr size_t WS_KN = WS_KV + (size_t)NH * 64 * 3072;
constexpr size_t WS_O = WS_KN + 16384;
constexpr size_t WS_AQ = WS_O + (size_t)2 * S * D * 2;
constexpr size_t WS_END = WS_AQ + (size_t)NH * 15 * S * 8 + 4096;

DI unsigned pack2(float lo, float hi) {
  f32x2 v = {lo, hi};
  return __builtin_bit_cast(unsigned, __builtin_convertvector(v, bf16x2_t));
}
DI float rowmax16(float x) {
  x = fmaxf(x, __int_as_float(__builtin_amdgcn_update_dpp(0, __float_as_int(x), 0x111, 0xf, 0xf, true)));
  x = fmaxf(x, __int_as_float(__builtin_amdgcn_update_dpp(0, __float_as_int(x), 0x112, 0xf, 0xf, true)));
  x = fmaxf(x, __int_as_float(__builtin_amdgcn_update_dpp(0, __float_as_int(x), 0x114, 0xf, 0xf, true)));
  x = fmaxf(x, __int_as_float(__builtin_amdgcn_update_dpp(0, __float_as_int(x), 0x118, 0xf, 0xf, true)));
  return x;
}
DI float lanef(float x, int l) { return __int_as_float(__builtin_amdgcn_readlane(__float_as_int(x), l)); }
DI float dot2(unsigned a, unsigned b, float c) {
  return __builtin_amdgcn_fdot2_f32_bf16(__builtin_bit_cast(bf16x2_t, a), __builtin_bit_cast(bf16x2_t, b), c, false);
}
DI float bf_lo(unsigned u) { return __uint_as_float(u << 16); }
DI float bf_hi(unsigned u) { return __uint_as_float(u & 0xffff0000u); }
DI void glds16(const char* base, unsigned voff, unsigned lds_a) {
  unsigned keep;
  const uint64_t b64 = (uint64_t)base;
  const uint64_t sbase = ((uint64_t)(unsigned)__builtin_amdgcn_readfirstlane((unsigned)(b64 >> 32)) << 32) |
                         (unsigned)__builtin_amdgcn_readfirstlane((unsigned)b64);
  const unsigned lds_addr = __builtin_amdgcn_readfirstlane(lds_a);
  asm volatile("s_mov_b32 %0, m0\n\ts_mov_b32 m0, %3\n\ts_nop 0\n\tglobal_load_lds_dwordx4 %1, %2\n\ts_mov_b32 m0, %0"
               : "=&s"(keep) : "v"(voff), "s"(sbase), "s"(lds_addr) : "memory");
}
#define SBAR() __builtin_amdgcn_sched_barrier(0)
#define PIN(x) asm volatile("" : "+v"(x))
#define KEEP(x) asm volatile("" :: "v"(x))
DI bf16x4 tr_read(const char* p) {
  return __builtin_bit_cast(bf16x4, __builtin_amdgcn_ds_read_tr16_b64_v4i16(LDS_PTR(v4i16_t, p)));
}
#define ST16(p, v) __builtin_nontemporal_store((u32x4)(v), (u32x4*)(p))
#define WAIT_VM(n) asm volatile("s_waitcnt vmcnt(" #n ")" ::: "memory")
#define WAIT_LGKM0() __builtin_amdgcn_s_waitcnt(0xC07F)
#define RAW_BAR() do { asm volatile("" ::: "memory"); WAIT_LGKM0(); __builtin_amdgcn_s_barrier(); asm volatile("" ::: "memory"); } while (0)

#define CODE_ADDR(sym, lo, hi)                                                                              \
  asm volatile("s_getpc_b64 s[90:91]\n\ts_add_u32 s90, s90, " sym "@rel32@lo+4\n\ts_addc_u32 s91, s91, " sym \
               "@rel32@hi+12\n\ts_mov_b32 %0, s90\n\ts_mov_b32 %1, s91" : "=s"(lo), "=s"(hi) :: "s90", "s91", "scc")

template <int BM, int NBUF>
DI void gemm_loader(const char* X, const char* W, unsigned ldw, int rot, char* lds, int w, int lane) {
  constexpr int XB = BM * 128, STAGE = XB + 64 * 256, NX = BM / 32, NP = NX + 4;
  __builtin_amdgcn_s_setprio(3);
  unsigned src[NP], dst[NP];
  const unsigned lds0 = (unsigned)(uintptr_t)lds;
#pragma unroll
  for (int j = 0; j < NX; ++j) {
    const int i = w + 4 * j, row = i * 8 + (lane >> 3), c = (lane & 7) ^ (row & 7);
    src[j] = row * (D * 2) + c * 16;
    dst[j] = lds0 + i * 1024;
  }
#pragma unroll
  for (int j = 0; j < 4; ++j) {
    const int i = w + 4 * j, rho = i * 4 + (lane >> 4), c16 = lane & 15;
    const int k = (rho & ~12) | ((rho & 4) << 1) | ((rho & 8) >> 1);
    src[NX + j] = k * ldw + (((c16 >> 1) ^ (rho & 7)) << 5) + (c16 & 1) * 16;
    dst[NX + j] = lds0 + XB + i * 1024;
  }
  auto stage = [&](int kt, int buf) {
    int k = kt + rot; if (k >= 10) k -= 10;
    const char* xs = X + k * 128;
    const char* wsrc = W + (size_t)k * 64 * ldw;
#pragma unroll
    for (int j = 0; j < 4; ++j) glds16(wsrc, src[NX + j], dst[NX + j] + buf * STAGE);
#pragma unroll
    for (int j = 0; j < NX; ++j) glds16(xs, src[j], dst[j] + buf * STAGE);
  };
  auto wait_stages = [&](int n) {
    if (n <= 0) WAIT_VM(0);
    else if (n == 1) { if (NP == 12) WAIT_VM(12); else WAIT_VM(8); }
    else if (n == 2) { if (NP == 12) WAIT_VM(24); else WAIT_VM(16); }
    else if (n == 3) { if (NP == 12) WAIT_VM(36); else WAIT_VM(24); }
    else WAIT_VM(32);
  };
#pragma unroll
  for (int i = 0; i < NBUF; ++i) stage(i, i);
  wait_stages(NBUF - 1);
  __builtin_amdgcn_s_barrier();
  int buf = 0;
#pragma unroll 1
  for (int kt = 0; kt < 10; ++kt) {
    wait_stages((kt + NBUF - 1 < 9 ? kt + NBUF - 1 : 9) - (kt + 1));
    __builtin_amdgcn_s_barrier();
    if (kt + NBUF < 10) stage(kt + NBUF, buf);
    buf = buf == NBUF - 1 ? 0 : buf + 1;
  }
}

template <int BM, int NBUF, bool SWAP>
DI void gemm_core(char* lds, int w, int lane, f32x4 (&acc)[4][BM / 64]) {
  constexpr int MB = BM / 64, XB = BM * 128, STAGE = XB + 64 * 256;
  const int wm = w >> 1, wn = w & 1, l15 = lane & 15, kq = lane >> 4;
  int xoff[MB][2], woff[4];
#pragma unroll
  for (int mb = 0; mb < MB; ++mb) {
    const int row = wm * (BM / 4) + mb * 16 + l15;
#pragma unroll
    for (int ks = 0; ks < 2; ++ks) xoff[mb][ks] = row * 128 + (((ks * 4 + kq) ^ (row & 7)) << 4);
  }
  {
    const int r7 = (kq & 1) * 4 + (l15 >> 2);
#pragma unroll
    for (int nb = 0; nb < 4; ++nb) woff[nb] = XB + ((kq >> 1) * 16 + r7) * 256 + (((wn * 4 + nb) ^ r7) << 5) + (l15 & 3) * 8;
  }
#pragma unroll
  for (int a = 0; a < 4; ++a)
#pragma unroll
    for (int b = 0; b < MB; ++b) acc[a][b] = (f32x4){0.f, 0.f, 0.f, 0.f};
  bf16x8 xf[2][MB], wf[2][4];
  auto rd = [&](const char* b, int ks) {
#pragma unroll
    for (int mb = 0; mb < MB; ++mb) xf[ks][mb] = *LDS_PTR(const bf16x8, b + xoff[mb][ks]);
#pragma unroll
    for (int nb = 0; nb < 4; ++nb) {
      const bf16x4 lo = tr_read(b + woff[nb] + ks * (32 * 256));
      const bf16x4 hi = tr_read(b + woff[nb] + ks * (32 * 256) + 8 * 256);
      wf[ks][nb] = __builtin_shufflevector(lo, hi, 0, 1, 2, 3, 4, 5, 6, 7);
    }
  };
  auto mm = [&](int ks) {
#pragma unroll
    for (int nb = 0; nb < 4; ++nb)
#pragma unroll
      for (int mb = 0; mb < MB; ++mb)
        acc[nb][mb] = SWAP ? __builtin_amdgcn_mfma_f32_16x16x32_bf16(xf[ks][mb], wf[ks][nb], acc[nb][mb], 0, 0, 0)
                           : __builtin_amdgcn_mfma_f32_16x16x32_bf16(wf[ks][nb], xf[ks][mb], acc[nb][mb], 0, 0, 0);
  };
  auto interleave = [&]() {
#pragma unroll
    for (int i = 0; i < 4 * MB; ++i) {
      __builtin_amdgcn_sched_group_barrier(0x008, 1, 0);
      __builtin_amdgcn_sched_group_barrier(0x100, MB == 4 ? 1 : 2, 0);
    }
  };
  RAW_BAR();
  rd(lds, 0);
  int buf = 0;
#pragma unroll 1
  for (int kt = 0; kt < 10; ++kt) {
    const char* b = lds + buf * STAGE;
    buf = buf == NBUF - 1 ? 0 : buf + 1;
    WAIT_LGKM0();
    SBAR();
    rd(b, 1);
    mm(0);
    interleave();
    SBAR();
    RAW_BAR();
    rd(lds + buf * STAGE, 0);
    mm(1);
    interleave();
    SBAR();
  }
  WAIT_LGKM0();
  asm volatile("" ::: "memory");
}

constexpr int K1_LDS = 3 * (256 * 128 + 64 * 256);

__global__ __launch_bounds__(768, 3) void k1_qkv(const unsigned short* __restrict__ xA, const unsigned short* __restrict__ wA,
                                                 const unsigned short* __restrict__ xB, const unsigned short* __restrict__ wB,
                                                 char* __restrict__ ws) {
  extern __shared__ __attribute__((aligned(16))) char lds[];
  const int tid = threadIdx.x, lane = tid & 63;
  const int w = __builtin_amdgcn_readfirstlane(tid >> 6);
  const int xcd = blockIdx.x & 7, slot = blockIdx.x >> 3;
  const int mloc = slot / 15, nblk = slot - mloc * 15;
  const int prob = xcd >> 2, mblk = (xcd & 3) * 2 + mloc;
  const int rot = (nblk + 5 * mloc) % 10;
  const char* X = (const char*)(prob ? xB : xA) + (size_t)mblk * 256 * (D * 2);
  const char* W = (const char*)(prob ? wB : wA) + nblk * 256;
  const int seg = nblk / 5;
  const bool swap = prob && seg == 2;

  if (w >= 8) {
    unsigned alo, ahi, blo, bhi;
    CODE_ADDR("_Z6k1_qkvPKtS0_S0_S0_Pc", alo, ahi);
    CODE_ADDR("_Z7k3_projPKcPKtS2_Pt", blo, bhi);
    const unsigned off = min((unsigned)((w - 8) * 64 + lane) * 128u, blo - alo + 3584u);
    const unsigned pf = *(const unsigned*)((((uint64_t)ahi << 32) | alo) + off);
    asm volatile("" :: "v"(pf));
    gemm_loader<256, 3>(X, W, N3 * 2, rot, lds, w - 8, lane); return; }
  f32x4 acc[4][4];
  if (swap) gemm_core<256, 3, true>(lds, w, lane, acc);
  else gemm_core<256, 3, false>(lds, w, lane, acc);

  const int wm = w >> 1, wn = w & 1, l15 = lane & 15, kq = lane >> 4;
  const int c0 = (nblk - seg * 5) * 128 + wn * 64;
  const int row0 = mblk * 256 + wm * 64;
  char* st = lds + (w < 6 ? 2 * 49152 + w * 8192 : (w - 6) * 8192);
  if (!swap) {
    const float sc = (prob && seg == 0) ? QSCALE : 1.f;
#pragma unroll
    for (int nb = 0; nb < 4; ++nb)
#pragma unroll
      for (int mb = 0; mb < 4; ++mb) {
        const f32x4 v = acc[nb][mb] * sc;
        *LDS_PTR(u32x2, st + (nb * 4 + kq) * 512 + (mb * 16 + l15) * 8) = (u32x2){pack2(v[0], v[1]), pack2(v[2], v[3])};
      }
    WAIT_LGKM0();
    asm volatile("" ::: "memory");
    if (prob && seg == 1) {
      float* kn = (float*)(ws + WS_KN) + (row0 >> 6);
      u32x2 qv[16];
#pragma unroll
      for (int q = 0; q < 16; ++q) qv[q] = *LDS_PTR(const u32x2, st + q * 512 + lane * 8);
      float s = 0.f;
      int hcur = c0 / 20;
#pragma unroll
      for (int q = 0; q < 16; ++q) {
        const unsigned u0 = qv[q][0], u1 = qv[q][1];
        float x;
        x = bf_lo(u0); s = fmaf(x, x, s); x = bf_hi(u0); s = fmaf(x, x, s);
        x = bf_lo(u1); s = fmaf(x, x, s); x = bf_hi(u1); s = fmaf(x, x, s);
        const bool hend = c0 + q * 4 + 4 == (hcur + 1) * 20;
        if (hend || q == 15) {
          s = rowmax16(s);
          s = fmaxf(fmaxf(lanef(s, 15), lanef(s, 31)), fmaxf(lanef(s, 47), lanef(s, 63)));
          const bool first = hcur * 20 >= c0;
          if (lane == 0) {
            kn[(hcur * 2 + (first ? 0 : 1)) * 32] = s;
            if (first && hend) kn[(hcur * 2 + 1) * 32] = 0.f;
          }
          s = 0.f; ++hcur;
        }
      }
    }
    const int tp = lane & 31;
#pragma unroll
    for (int it = 0; it < 8; ++it) {
      const u32x4 v = *LDS_PTR(const u32x4, st + it * 1024 + lane * 16);
      const int q = (c0 >> 2) + it * 2 + (lane >> 5), hh = q / 5, dq = q - hh * 5;
      if (!prob) {
        ST16(ws + WS_AQ + ((size_t)(hh * 15 + seg * 5 + dq) * S + row0 + 2 * tp) * 8, v);
      } else {
        char* g = ws + (seg ? WS_KV : WS_QB) + ((size_t)hh * 64 + (row0 >> 5) + (tp >> 4)) * (seg ? 3072 : 1536) + dq * 256 + (tp & 15) * 16;
        ST16(g, v);
        if (seg && dq == 4) ST16(g + 256, ((u32x4){0x3F80u, 0u, 0x3F80u, 0u}));
      }
    }
  } else {
#pragma unroll
    for (int nb = 0; nb < 4; ++nb)
#pragma unroll
      for (int mb = 0; mb < 4; ++mb) {
        const f32x4 v = acc[nb][mb];
        *LDS_PTR(u32x2, st + (mb * 2 + (kq & 1)) * 1024 + (nb * 16 + l15) * 16 + (kq >> 1) * 8) = (u32x2){pack2(v[0], v[1]), pack2(v[2], v[3])};
      }
    WAIT_LGKM0();
    asm volatile("" ::: "memory");
    const int c = c0 + lane, hh = c / 20, d = c - hh * 20;
    char* g = ws + WS_KV + ((size_t)hh * 64 + (row0 >> 5)) * 3072 + 1536 + d * 16;
#pragma unroll
    for (int it = 0; it < 8; ++it) {
      const u32x4 v = *LDS_PTR(const u32x4, st + it * 1024 + lane * 16);
      ST16(g + (it >> 2) * 3072 + (it & 3) * 384, v);
    }
#pragma unroll
    for (int it = 0; it < 2; ++it) {
      const int h2 = (c0 + 19) / 20 + it * 2 + (lane >> 5), blk = (lane >> 4) & 1, sh = (lane >> 2) & 3, d2 = 20 + (lane & 3);
      const unsigned o = d2 == 20 ? 0x3F803F80u : 0u;
      if (h2 * 20 < c0 + 64)
        ST16(ws + WS_KV + ((size_t)h2 * 64 + (row0 >> 5) + blk) * 3072 + 1536 + sh * 384 + d2 * 16, ((u32x4){o, o, o, o}));
    }
  }

}

constexpr int K2_HALFB = 8 * 3072;
constexpr int K2_STAGE = 2 * K2_HALFB;
constexpr int K2_LOC = 15 * 640;
constexpr int K2_LDS = 3 * K2_STAGE;
#define MFMA32(a, b, c) __builtin_amdgcn_mfma_f32_32x32x16_bf16(a, b, c, 0, 0, 0)

DI bf16x8 cat44(bf16x4 a, bf16x4 b) { return __builtin_shufflevector(a, b, 0, 1, 2, 3, 4, 5, 6, 7); }
DI void exp_pack(const f32x16& s, bf16x8& p0, bf16x8& p1) {
  u32x4 a, b;
#pragma unroll
  for (int i = 0; i < 4; ++i) {
    a[i] = pack2(__builtin_amdgcn_exp2f(s[2 * i]), __builtin_amdgcn_exp2f(s[2 * i + 1]));
    b[i] = pack2(__builtin_amdgcn_exp2f(s[8 + 2 * i]), __builtin_amdgcn_exp2f(s[8 + 2 * i + 1]));
  }
  p0 = __builtin_bit_cast(bf16x8, a);
  p1 = __builtin_bit_cast(bf16x8, b);
}
DI float swap32(float x) {
  const auto r = __builtin_amdgcn_permlane32_swap(__float_as_uint(x), __float_as_uint(x), false, false);
  return __uint_as_float((threadIdx.x & 32) ? r[0] : r[1]);
}
DI float sumsq8(bf16x8 f) {
  const u32x4 u = __builtin_bit_cast(u32x4, f);
  float s = 0.f;
#pragma unroll
  for (int i = 0; i < 4; ++i) { float x = bf_lo(u[i]); s = fmaf(x, x, s); x = bf_hi(u[i]); s = fmaf(x, x, s); }
  return s;
}

DI void attn_fallback(const char* kvh, bf16x8 q0, bf16x8 q1, int lane, f32x16& O) {
  const int r32 = lane & 31, h = lane >> 5;
  const int dv = r32 < 24 ? r32 : (r32 < 28 ? r32 - 4 : r32 - 12);
  float m = -INFINITY;
  O = (f32x16){};
#pragma unroll 1
  for (int t = 0; t < 64; ++t) {
    const char* g = kvh + (size_t)t * 3072;
    const bf16x8 k0 = cat44(*(const bf16x4*)(g + h * 512 + r32 * 8), *(const bf16x4*)(g + h * 512 + 256 + r32 * 8));
    const bf16x8 k1 = cat44(*(const bf16x4*)(g + 1024 + r32 * 8), *(const bf16x4*)(g + 1280 + r32 * 8));
    const bf16x8 v0 = *(const bf16x8*)(g + 1536 + h * 384 + dv * 16);
    const bf16x8 v1 = *(const bf16x8*)(g + 1536 + 768 + h * 384 + dv * 16);
    f32x16 s = MFMA32(k0, q0, (f32x16){});
    s = MFMA32(k1, q1, s);
    float pm = s[0];
#pragma unroll
    for (int i = 1; i < 16; ++i) pm = fmaxf(pm, s[i]);
    pm = fmaxf(pm, swap32(pm));
    const float mn = fmaxf(m, pm);
    const float alpha = __builtin_amdgcn_exp2f(m - mn);
    m = mn;
#pragma unroll
    for (int i = 0; i < 16; ++i) { s[i] -= mn; O[i] *= alpha; }
    bf16x8 p0, p1;
    exp_pack(s, p0, p1);
    O = MFMA32(v0, p0, O);
    O = MFMA32(v1, p1, O);
  }
}

template <bool MASK>
DI void local_attn(const char* me, int s, float (&o)[20]) {
  u32x2 q[5], kv[8][5];
#pragma unroll
  for (int i = 0; i < 5; ++i) q[i] = *LDS_PTR(const u32x2, me + i * 640);
#pragma unroll
  for (int j = 0; j < 8; ++j) {
    const char* kr = me - (MASK ? min(2 * j, s) : 2 * j) * 8 + 5 * 640;
#pragma unroll
    for (int i = 0; i < 5; ++i) kv[j][i] = *LDS_PTR(const u32x2, kr + i * 640);
  }
  SBAR();
  float lg[8];
  float mx = -INFINITY;
#pragma unroll
  for (int j = 0; j < 8; ++j) {
    float a0 = 0.f, a1 = 0.f;
#pragma unroll
    for (int i = 0; i < 5; ++i) {
      const unsigned x0 = kv[j][i][0], x1 = kv[j][i][1], y0 = q[i][0], y1 = q[i][1];
      a0 = dot2(x0, y0, a0);
      a1 = dot2(x1, y1, a1);
    }
    lg[j] = (a0 + a1) * QSCALE;
    if (MASK && s < 2 * j) lg[j] = -INFINITY;
    mx = fmaxf(mx, lg[j]);
  }
  SBAR();
#pragma unroll
  for (int j = 0; j < 8; ++j) {
    const char* vr = me - (MASK ? min(2 * j, s) : 2 * j) * 8 + 10 * 640;
#pragma unroll
    for (int i = 0; i < 5; ++i) kv[j][i] = *LDS_PTR(const u32x2, vr + i * 640);
  }
  SBAR();
  float sum = 0.f;
#pragma unroll
  for (int j = 0; j < 8; ++j) { lg[j] = __builtin_amdgcn_exp2f(lg[j] - mx); sum += lg[j]; }
  const float inv = 1.f / sum;
#pragma unroll
  for (int i = 0; i < 20; ++i) o[i] = 0.f;
#pragma unroll
  for (int j = 0; j < 8; ++j) {
    const unsigned pl = pack2(lg[j] * inv, 0.f), ph = pl << 16;
#pragma unroll
    for (int i = 0; i < 5; ++i) {
      const unsigned x0 = kv[j][i][0], x1 = kv[j][i][1];
      o[4 * i + 0] = dot2(x0, pl, o[4 * i + 0]);
      o[4 * i + 1] = dot2(x0, ph, o[4 * i + 1]);
      o[4 * i + 2] = dot2(x1, pl, o[4 * i + 2]);
      o[4 * i + 3] = dot2(x1, ph, o[4 * i + 3]);
    }
  }
}

__global__ __launch_bounds__(768, 3) void k2_attn(char* __restrict__ ws, const char* __restrict__ woA, const char* __restrict__ woB) {
  extern __shared__ __attribute__((aligned(16))) char lds[];
  const int tid = threadIdx.x, lane = tid & 63;
  const int w = __builtin_amdgcn_readfirstlane(tid >> 6);

  if (w >= 8) {
    __builtin_amdgcn_s_setprio(3);
    const int task = blockIdx.x * 4 + (w - 8), hA = task >> 5, r0 = (task & 31) * 64;
    char* L = lds + 2 * K2_STAGE + (w - 8) * K2_LOC;
    const char* g = ws + WS_AQ + ((ptrdiff_t)hA * 15 * S + r0 - 16) * 8;
    __builtin_amdgcn_s_barrier();
    if (lane < 40) {
#pragma unroll
      for (int i = 0; i < 15; ++i) glds16(g + (size_t)i * S * 8, lane * 16, (unsigned)(uintptr_t)L + i * 640);
    }
    WAIT_VM(0);

    const unsigned line = min(((blockIdx.x >> 3) * 4 + (w - 8)) * 64 + lane, 6399u);
    const unsigned pf = *(const unsigned*)(((blockIdx.x & 4) ? woB : woA) + (size_t)line * 128);
    SBAR();
    const int s = r0 + lane;
    float o[20];
    if (r0 == 0) local_attn<true>(L + (lane + 16) * 8, s, o); else local_attn<false>(L + (lane + 16) * 8, s, o);
    char* og = ws + WS_O + (size_t)s * (D * 2) + hA * 40;
#pragma unroll
    for (int i = 0; i < 5; ++i) *(u32x2*)(og + i * 8) = (u32x2){pack2(o[4 * i], o[4 * i + 1]), pack2(o[4 * i + 2], o[4 * i + 3])};
    asm volatile("" :: "v"(pf));

    return;
  }

  const int xcd = blockIdx.x & 7, slot = blockIdx.x >> 3;
  const int head = xcd * 4 + (slot >> 3), qc = slot & 7;
  const int qg = w & 3, kh = w >> 2;
  const int r32 = lane & 31, h = lane >> 5;
  const char* kvh = ws + WS_KV + (size_t)head * 64 * 3072;

  const int dhalf = w >> 2, doff = (w & 3) * 6144;
  const char* dsrc = kvh + (size_t)dhalf * 32 * 3072 + doff;
  const unsigned ddst = (unsigned)(uintptr_t)lds + dhalf * K2_HALFB + doff;
  auto stage = [&](int i) {
    const unsigned bo = (i == 3 ? 0 : i) * K2_STAGE;
#pragma unroll
    for (int j = 0; j < 6; ++j) glds16(dsrc + (size_t)i * K2_HALFB + j * 1024, lane * 16, ddst + bo + j * 1024);
  };
  stage(0);
  stage(1);

  bf16x8 qa0, qa1, qb0, qb1;
  {
    const char* qg_ = ws + WS_QB + ((size_t)head * 64 + qc * 8 + qg * 2) * 1536;
    const bf16x4 z4 = {};
    qa0 = cat44(*(const bf16x4*)(qg_ + h * 512 + r32 * 8), *(const bf16x4*)(qg_ + h * 512 + 256 + r32 * 8));
    qa1 = cat44(*(const bf16x4*)(qg_ + 1024 + r32 * 8), z4);
    qb0 = cat44(*(const bf16x4*)(qg_ + 1536 + h * 512 + r32 * 8), *(const bf16x4*)(qg_ + 1536 + h * 512 + 256 + r32 * 8));
    qb1 = cat44(*(const bf16x4*)(qg_ + 1536 + 1024 + r32 * 8), z4);
    float kn = rowmax16(((const float*)(ws + WS_KN))[head * 64 + lane]);
    kn = fmaxf(lanef(kn, 15), lanef(kn, 31)) + fmaxf(lanef(kn, 47), lanef(kn, 63));
    if (h) { qa1 = (bf16x8){}; qb1 = (bf16x8){}; }
    float na = sumsq8(qa0) + sumsq8(qa1), nb = sumsq8(qb0) + sumsq8(qb1);
    na += swap32(na);
    nb += swap32(nb);
    const float ra = sqrtf(na * kn) * 1.01f - 31.f, rb = sqrtf(nb * kn) * 1.01f - 31.f;
    if (!h) {
      qa1[4] = (short)(pack2(-ra, 0.f) & 0xffff);
      qb1[4] = (short)(pack2(-rb, 0.f) & 0xffff);
    }
  }
  const int dv = min(r32, 21);
  const int koff0 = kh * K2_HALFB + h * 512 + r32 * 8, koff1 = kh * K2_HALFB + 1024 + r32 * 8;
#define LDK(p) cat44(*LDS_PTR(const bf16x4, (p)), *LDS_PTR(const bf16x4, (p) + 256))
  const int voff0 = kh * K2_HALFB + 1536 + h * 384 + dv * 16, voff1 = voff0 + 768;

  f32x16 Oa = {}, Ob = {}, Sa, Sb;
  const f32x16 zero = {};
  bf16x8 kf[2][2], vf[2][2];
  WAIT_VM(6);
  RAW_BAR();

  __builtin_amdgcn_s_setprio(3);
  kf[0][0] = LDK(lds + koff0);
  kf[0][1] = LDK(lds + koff1);
  vf[0][0] = *LDS_PTR(const bf16x8, lds + voff0);
  vf[0][1] = *LDS_PTR(const bf16x8, lds + voff1);
  Sa = MFMA32(kf[0][0], qa0, zero);
  Sa = MFMA32(kf[0][1], qa1, Sa);
  u32x4 pk = {};
  u32x4 pc = {};
  float e12 = 0.f, e13 = 0.f, e14 = 0.f, e15 = 0.f;
  bf16x8 vlast = {};
#define EXP2(x) __builtin_amdgcn_exp2f(x)
#define BC8(x) __builtin_bit_cast(bf16x8, x)
#define E_(d, x) d = EXP2(x); PIN(d); SBAR();
#define C_(v, i, x, y) v[i] = pack2(x, y); PIN(v); SBAR();
#define HALF(SC, OC, OP, VP, SN, KN0, KN1, QN0, QN1, V0)                                         \
  do {                                                                                           \
    u32x4 p0;                                                                                    \
    float a0, a1, a2, a3, a4, a5, a6, a7, a8, a9, a10, a11;                                      \
    SBAR();                                                                                      \
    SN = MFMA32(KN0, QN0, zero); PIN(SN); SBAR();                                                \
    E_(a0, SC[0]) C_(pc, 2, e12, e13) E_(a1, SC[1]) C_(pc, 3, e14, e15) E_(a2, SC[2]) E_(a3, SC[3]) \
    OP = MFMA32(VP, BC8(pc), OP); PIN(OP); KEEP(pk); SBAR();                                     \
    E_(a4, SC[4]) E_(a5, SC[5]) C_(p0, 0, a0, a1) E_(a6, SC[6]) E_(a7, SC[7]) C_(p0, 1, a2, a3)  \
    SN = MFMA32(KN1, QN1, SN); PIN(SN); KEEP(pc); SBAR();                                        \
    E_(a8, SC[8]) C_(p0, 2, a4, a5) E_(a9, SC[9]) C_(p0, 3, a6, a7) E_(a10, SC[10]) E_(a11, SC[11]) \
    OC = MFMA32(V0, BC8(p0), OC); PIN(OC); SBAR();                                               \
    E_(e12, SC[12]) C_(pc, 0, a8, a9) E_(e13, SC[13]) C_(pc, 1, a10, a11) E_(e14, SC[14]) E_(e15, SC[15]) \
    pk = p0;                                                                                     \
  } while (0)
#pragma unroll 1
  for (int i = 0; i < 8; ++i) {
    const int sg = i >> 1;
    const char* b = lds + (sg == 3 ? 0 : sg) * K2_STAGE + (i & 1) * (4 * 3072);
    const char* bn = (i & 1) ? lds + (sg >= 2 ? 0 : sg + 1) * K2_STAGE : b + 4 * 3072;
#pragma unroll
    for (int bb = 0; bb < 4; ++bb) {
      const int c = bb & 1, n = c ^ 1;
      if (bb == 3 && (i & 1)) {
        WAIT_VM(0);
        RAW_BAR();
        if (sg + 2 < 4) stage(sg + 2);
      }
      if (bb == 3) { if (i & 1) __builtin_amdgcn_s_setprio(3); else __builtin_amdgcn_s_setprio(1); }
      else if (bb == 1) { if (i & 1) __builtin_amdgcn_s_setprio(0); else __builtin_amdgcn_s_setprio(2); }
      const char* nx = bb == 3 ? bn : b + (bb + 1) * 3072;
      kf[n][0] = LDK(nx + koff0);
      kf[n][1] = LDK(nx + koff1);
      HALF(Sa, Oa, Ob, vlast, Sb, kf[c][0], kf[c][1], qb0, qb1, vf[c][0]);
      vf[n][0] = *LDS_PTR(const bf16x8, nx + voff0);
      vf[n][1] = *LDS_PTR(const bf16x8, nx + voff1);
      HALF(Sb, Ob, Oa, vf[c][1], Sa, kf[n][0], kf[n][1], qa0, qa1, vf[c][0]);
      vlast = vf[c][1];
    }
  }
  pc[2] = pack2(e12, e13);
  pc[3] = pack2(e14, e15);
  Ob = MFMA32(vlast, BC8(pc), Ob);

  RAW_BAR();
  {
    float* ex = (float*)lds + (size_t)((qg * 2 + kh) * 16) * 64 + lane;
#pragma unroll
    for (int r = 0; r < 16; ++r) *LDS_PTR(float, ex + r * 64) = kh ? Oa[r] : Ob[r];
  }
  __syncthreads();
  f32x16 O;
  {
    const float* ex = (const float*)lds + (size_t)((qg * 2 + (kh ^ 1)) * 16) * 64 + lane;
#pragma unroll
    for (int r = 0; r < 16; ++r) O[r] = (kh ? Ob[r] : Oa[r]) + *LDS_PTR(const float, ex + r * 64);
  }
  float l = h ? O[8] : 0.f;
  l += swap32(l);
  float amax = fabsf(l);
#pragma unroll
  for (int r = 0; r < 12; ++r) amax = fmaxf(amax, fabsf(O[r]));
  const bool bad = !(l >= 8.4703294725430034e-22f  ) || !(amax <= 3.0e38f);
  if (__any(bad)) {
    bf16x8 f0 = kh ? qb0 : qa0, f1 = kh ? qb1 : qa1;
    if (!h) f1[4] = 0;
    attn_fallback(kvh, f0, f1, lane, O);
    l = h ? O[8] : 0.f;
    l += swap32(l);
  }
  const float inv = 1.f / l;
  const int q = (qc * 8 + qg * 2 + kh) * 32 + r32;
  char* og = ws + WS_O + ((size_t)S + q) * (D * 2) + head * 40 + h * 8;
#pragma unroll
  for (int g = 0; g < 3; ++g) {
    if (g < 2 || !h)
      *(u32x2*)(og + g * 16) = (u32x2){pack2(O[4 * g] * inv, O[4 * g + 1] * inv), pack2(O[4 * g + 2] * inv, O[4 * g + 3] * inv)};
  }

}

constexpr int K3_LDS = 5 * (128 * 128 + 64 * 256);

__global__ __launch_bounds__(768, 3) void k3_proj(const char* __restrict__ ws, const unsigned short* __restrict__ woA,
                                                  const unsigned short* __restrict__ woB, unsigned short* __restrict__ out) {
  extern __shared__ __attribute__((aligned(16))) char lds[];
  const int tid = threadIdx.x, lane = tid & 63;
  const int w = __builtin_amdgcn_readfirstlane(tid >> 6);
  const int xcd = blockIdx.x & 7, slot = blockIdx.x >> 3;
  const int mloc = slot / 5, nblk = slot - mloc * 5;
  const int prob = xcd >> 2, mblk = (xcd & 3) * 4 + mloc;
  const int rot = (2 * nblk + 3 * mloc) % 10;
  const char* X = ws + WS_O + ((size_t)prob * S + mblk * 128) * (D * 2);
  const char* W = (const char*)(prob ? woB : woA) + nblk * 256;

  if (w >= 8) { gemm_loader<128, 5>(X, W, D * 2, rot, lds, w - 8, lane); return; }
  f32x4 acc[4][2];
  gemm_core<128, 5, false>(lds, w, lane, acc);
  const int wm = w >> 1, wn = w & 1, l15 = lane & 15, kq = lane >> 4;
  char* st = lds + 2 * 32768 + w * 4096;
#pragma unroll
  for (int nb = 0; nb < 4; ++nb)
#pragma unroll
    for (int mb = 0; mb < 2; ++mb) {
      const f32x4 v = acc[nb][mb];
      const int r = mb * 16 + l15;
      *LDS_PTR(u32x2, st + r * 128 + (((nb * 2 + (kq >> 1)) ^ (r & 7)) << 4) + (kq & 1) * 8) = (u32x2){pack2(v[0], v[1]), pack2(v[2], v[3])};
    }
  WAIT_LGKM0();
  asm volatile("" ::: "memory");
#pragma unroll
  for (int it = 0; it < 4; ++it) {
    const int r = it * 8 + (lane >> 3), c = lane & 7;
    const u32x4 v = *LDS_PTR(const u32x4, st + r * 128 + ((c ^ (r & 7)) << 4));
    ST16(out + (size_t)(mblk * 128 + wm * 32 + r) * (2 * D) + prob * D + nblk * 128 + wn * 64 + c * 8, v);
  }

}

extern "C" void kernel_launch(void* const* d_in, const int* in_sizes, int n_in,
                              void* d_out, int out_size, void* d_ws, size_t ws_size,
                              hipStream_t stream) {
  (void)in_sizes; (void)n_in; (void)out_size; (void)ws_size;
  const unsigned short* xA = (const unsigned short*)d_in[0];
  const unsigned short* wA = (const unsigned short*)d_in[1];
  const unsigned short* oA = (const unsigned short*)d_in[2];
  const unsigned short* xB = (const unsigned short*)d_in[3];
  const unsigned short* wB = (const unsigned short*)d_in[4];
  const unsigned short* oB = (const unsigned short*)d_in[5];
  char* ws = (char*)d_ws;
  (void)hipFuncSetAttribute((const void*)k1_qkv, hipFuncAttributeMaxDynamicSharedMemorySize, K1_LDS);
  (void)hipFuncSetAttribute((const void*)k2_attn, hipFuncAttributeMaxDynamicSharedMemorySize, K2_LDS);
  (void)hipFuncSetAttribute((const void*)k3_proj, hipFuncAttributeMaxDynamicSharedMemorySize, K3_LDS);
  hipLaunchKernelGGL(k1_qkv, dim3(240), dim3(768), K1_LDS, stream, xA, wA, xB, wB, ws);
  hipLaunchKernelGGL(k2_attn, dim3(256), dim3(768), K2_LDS, stream, ws, (const char*)oA, (const char*)oB);
  hipLaunchKernelGGL(k3_proj, dim3(160), dim3(768), K3_LDS, stream, (const char*)ws, oA, oB, (unsigned short*)d_out);
}
```

```cpp
#include <hip/hip_runtime.h>
#include <hip/hip_bf16.h>
#include <stdint.h>

typedef __attribute__((ext_vector_type(8))) short bf16x8;
typedef __attribute__((ext_vector_type(4))) short bf16x4;
typedef __attribute__((ext_vector_type(4))) float f32x4;
typedef __attribute__((ext_vector_type(2))) float f32x2;
typedef __attribute__((ext_vector_type(16))) float f32x16;
typedef __attribute__((ext_vector_type(4))) unsigned u32x4;
typedef __attribute__((ext_vector_type(2))) unsigned u32x2;
typedef __attribute__((ext_vector_type(2))) __bf16 bf16x2_t;
typedef __attribute__((address_space(3))) const char* lds_cptr;
typedef short v4i16_t __attribute__((ext_vector_type(4)));

#define DI __device__ __forceinline__
#define LDS_PTR(T, p) ((__attribute__((address_space(3))) T*)(p))

constexpr int S = 2048, D = 640, N3 = 1920, NH = 32, DH = 20;
constexpr float SM_SCALE = 0.22360679774997896f;
constexpr float LOG2E = 1.4426950408889634f;
constexpr float QSCALE = SM_SCALE * LOG2E;

constexpr size_t WS_QB = 0;
constexpr size_t WS_KV = WS_QB + (size_t)NH * 64 * 1536;
constexpr size_t WS_KN = WS_KV + (size_t)NH * 64 * 3072;
constexpr size_t WS_O = WS_KN + 16384;
constexpr size_t WS_AQ = WS_O + (size_t)2 * S * D * 2;
constexpr size_t WS_END = WS_AQ + (size_t)NH * 15 * S * 8 + 4096;

DI unsigned pack2(float lo, float hi) {
  f32x2 v = {lo, hi};
  return __builtin_bit_cast(unsigned, __builtin_convertvector(v, bf16x2_t));
}
DI float rowmax16(float x) {
  x = fmaxf(x, __int_as_float(__builtin_amdgcn_update_dpp(0, __float_as_int(x), 0x111, 0xf, 0xf, true)));
  x = fmaxf(x, __int_as_float(__builtin_amdgcn_update_dpp(0, __float_as_int(x), 0x112, 0xf, 0xf, true)));
  x = fmaxf(x, __int_as_float(__builtin_amdgcn_update_dpp(0, __float_as_int(x), 0x114, 0xf, 0xf, true)));
  x = fmaxf(x, __int_as_float(__builtin_amdgcn_update_dpp(0, __float_as_int(x), 0x118, 0xf, 0xf, true)));
  return x;
}
DI float lanef(float x, int l) { return __int_as_float(__builtin_amdgcn_readlane(__float_as_int(x), l)); }
DI float dot2(unsigned a, unsigned b, float c) {
  return __builtin_amdgcn_fdot2_f32_bf16(__builtin_bit_cast(bf16x2_t, a), __builtin_bit_cast(bf16x2_t, b), c, false);
}
DI float bf_lo(unsigned u) { return __uint_as_float(u << 16); }
DI float bf_hi(unsigned u) { return __uint_as_float(u & 0xffff0000u); }
DI void glds16(const char* base, unsigned voff, unsigned lds_a) {
  unsigned keep;
  const uint64_t b64 = (uint64_t)base;
  const uint64_t sbase = ((uint64_t)(unsigned)__builtin_amdgcn_readfirstlane((unsigned)(b64 >> 32)) << 32) |
                         (unsigned)__builtin_amdgcn_readfirstlane((unsigned)b64);
  const unsigned lds_addr = __builtin_amdgcn_readfirstlane(lds_a);
  asm volatile("s_mov_b32 %0, m0\n\ts_mov_b32 m0, %3\n\ts_nop 3\n\tglobal_load_lds_dwordx4 %1, %2\n\ts_mov_b32 m0, %0"
               : "=&s"(keep) : "v"(voff), "s"(sbase), "s"(lds_addr) : "memory");
}
#define SBAR() __builtin_amdgcn_sched_barrier(0)
#define PIN(x) asm volatile("" : "+v"(x))
#define KEEP(x) asm volatile("" :: "v"(x))
DI bf16x4 tr_read(const char* p) {
  return __builtin_bit_cast(bf16x4, __builtin_amdgcn_ds_read_tr16_b64_v4i16(LDS_PTR(v4i16_t, p)));
}
DI void st16_wt(void* p, u32x4 v) { asm volatile("global_store_dwordx4 %0, %1, off sc0 sc1\n\ts_nop 1" :: "v"(p), "v"(v) : "memory"); }
#define ST16(p, v) st16_wt((void*)(p), (v))
#define WAIT_VM(n) asm volatile("s_waitcnt vmcnt(" #n ")" ::: "memory")
#define WAIT_LGKM0() __builtin_amdgcn_s_waitcnt(0xC07F)
#define RAW_BAR() do { asm volatile("" ::: "memory"); WAIT_LGKM0(); __builtin_amdgcn_s_barrier(); asm volatile("" ::: "memory"); } while (0)

#define CODE_ADDR(sym, lo, hi)                                                                              \
  asm volatile("s_getpc_b64 s[90:91]\n\ts_add_u32 s90, s90, " sym "@rel32@lo+4\n\ts_addc_u32 s91, s91, " sym \
               "@rel32@hi+12\n\ts_mov_b32 %0, s90\n\ts_mov_b32 %1, s91" : "=s"(lo), "=s"(hi) :: "s90", "s91", "scc")

template <int BM, int NBUF>
DI void gemm_loader(const char* X, const char* W, unsigned ldw, int rot, char* lds, int w, int lane) {
  constexpr int XB = BM * 128, STAGE = XB + 64 * 256, NX = BM / 32, NP = NX + 4;
  __builtin_amdgcn_s_setprio(3);
  unsigned src[NP], dst[NP];
  const unsigned lds0 = (unsigned)(uintptr_t)lds;
#pragma unroll
  for (int j = 0; j < NX; ++j) {
    const int i = w + 4 * j, row = i * 8 + (lane >> 3), c = (lane & 7) ^ (row & 7);
    src[j] = row * (D * 2) + c * 16;
    dst[j] = lds0 + i * 1024;
  }
#pragma unroll
  for (int j = 0; j < 4; ++j) {
    const int i = w + 4 * j, rho = i * 4 + (lane >> 4), c16 = lane & 15;
    const int k = (rho & ~12) | ((rho & 4) << 1) | ((rho & 8) >> 1);
    src[NX + j] = k * ldw + (((c16 >> 1) ^ (rho & 7)) << 5) + (c16 & 1) * 16;
    dst[NX + j] = lds0 + XB + i * 1024;
  }
  auto stage = [&](int kt, int buf) {
    int k = kt + rot; if (k >= 10) k -= 10;
    const char* xs = X + k * 128;
    const char* wsrc = W + (size_t)k * 64 * ldw;
#pragma unroll
    for (int j = 0; j < 4; ++j) glds16(wsrc, src[NX + j], dst[NX + j] + buf * STAGE);
#pragma unroll
    for (int j = 0; j < NX; ++j) glds16(xs, src[j], dst[j] + buf * STAGE);
  };
  auto wait_stages = [&](int n) {
    if (n <= 0) WAIT_VM(0);
    else if (n == 1) { if (NP == 12) WAIT_VM(12); else WAIT_VM(8); }
    else if (n == 2) { if (NP == 12) WAIT_VM(24); else WAIT_VM(16); }
    else if (n == 3) { if (NP == 12) WAIT_VM(36); else WAIT_VM(24); }
    else WAIT_VM(32);
  };
#pragma unroll
  for (int i = 0; i < NBUF; ++i) stage(i, i);
  wait_stages(NBUF - 1);
  __builtin_amdgcn_s_barrier();
  int buf = 0;
#pragma unroll 1
  for (int kt = 0; kt < 10; ++kt) {
    wait_stages((kt + NBUF - 1 < 9 ? kt + NBUF - 1 : 9) - (kt + 1));
    __builtin_amdgcn_s_barrier();
    if (kt + NBUF < 10) stage(kt + NBUF, buf);
    buf = buf == NBUF - 1 ? 0 : buf + 1;
  }
}

template <int BM, int NBUF, bool SWAP>
DI void gemm_core(char* lds, int w, int lane, f32x4 (&acc)[4][BM / 64]) {
  constexpr int MB = BM / 64, XB = BM * 128, STAGE = XB + 64 * 256;
  const int wm = w >> 1, wn = w & 1, l15 = lane & 15, kq = lane >> 4;
  int xoff[MB][2], woff[4];
#pragma unroll
  for (int mb = 0; mb < MB; ++mb) {
    const int row = wm * (BM / 4) + mb * 16 + l15;
#pragma unroll
    for (int ks = 0; ks < 2; ++ks) xoff[mb][ks] = row * 128 + (((ks * 4 + kq) ^ (row & 7)) << 4);
  }
  {
    const int r7 = (kq & 1) * 4 + (l15 >> 2);
#pragma unroll
    for (int nb = 0; nb < 4; ++nb) woff[nb] = XB + ((kq >> 1) * 16 + r7) * 256 + (((wn * 4 + nb) ^ r7) << 5) + (l15 & 3) * 8;
  }
#pragma unroll
  for (int a = 0; a < 4; ++a)
#pragma unroll
    for (int b = 0; b < MB; ++b) acc[a][b] = (f32x4){0.f, 0.f, 0.f, 0.f};
  bf16x8 xf[2][MB], wf[2][4];
  auto rd = [&](const char* b, int ks) {
#pragma unroll
    for (int mb = 0; mb < MB; ++mb) xf[ks][mb] = *LDS_PTR(const bf16x8, b + xoff[mb][ks]);
#pragma unroll
    for (int nb = 0; nb < 4; ++nb) {
      const bf16x4 lo = tr_read(b + woff[nb] + ks * (32 * 256));
      const bf16x4 hi = tr_read(b + woff[nb] + ks * (32 * 256) + 8 * 256);
      wf[ks][nb] = __builtin_shufflevector(lo, hi, 0, 1, 2, 3, 4, 5, 6, 7);
    }
  };
  auto mm = [&](int ks) {
#pragma unroll
    for (int nb = 0; nb < 4; ++nb)
#pragma unroll
      for (int mb = 0; mb < MB; ++mb)
        acc[nb][mb] = SWAP ? __builtin_amdgcn_mfma_f32_16x16x32_bf16(xf[ks][mb], wf[ks][nb], acc[nb][mb], 0, 0, 0)
                           : __builtin_amdgcn_mfma_f32_16x16x32_bf16(wf[ks][nb], xf[ks][mb], acc[nb][mb], 0, 0, 0);
  };
  auto interleave = [&]() {
#pragma unroll
    for (int i = 0; i < 4 * MB; ++i) {
      __builtin_amdgcn_sched_group_barrier(0x008, 1, 0);
      __builtin_amdgcn_sched_group_barrier(0x100, MB == 4 ? 1 : 2, 0);
    }
  };
  RAW_BAR();
  rd(lds, 0);
  int buf = 0;
#pragma unroll 1
  for (int kt = 0; kt < 10; ++kt) {
    const char* b = lds + buf * STAGE;
    buf = buf == NBUF - 1 ? 0 : buf + 1;
    WAIT_LGKM0();
    SBAR();
    rd(b, 1);
    mm(0);
    interleave();
    SBAR();
    RAW_BAR();
    rd(lds + buf * STAGE, 0);
    mm(1);
    interleave();
    SBAR();
  }
  WAIT_LGKM0();
  asm volatile("" ::: "memory");
}

constexpr int K1_LDS = 3 * (256 * 128 + 64 * 256);

__global__ __launch_bounds__(768, 3) void k1_qkv(const unsigned short* __restrict__ xA, const unsigned short* __restrict__ wA,
                                                 const unsigned short* __restrict__ xB, const unsigned short* __restrict__ wB,
                                                 char* __restrict__ ws) {
  extern __shared__ __attribute__((aligned(16))) char lds[];
  const int tid = threadIdx.x, lane = tid & 63;
  const int w = __builtin_amdgcn_readfirstlane(tid >> 6);
  const int xcd = blockIdx.x & 7, slot = blockIdx.x >> 3;
  const int mloc = slot / 15, nblk = slot - mloc * 15;
  const int prob = xcd >> 2, mblk = (xcd & 3) * 2 + mloc;
  const int rot = (nblk + 5 * mloc) % 10;
  const char* X = (const char*)(prob ? xB : xA) + (size_t)mblk * 256 * (D * 2);
  const char* W = (const char*)(prob ? wB : wA) + nblk * 256;
  const int seg = nblk / 5;
  const bool swap = prob && seg == 2;

  if (w >= 8) { gemm_loader<256, 3>(X, W, N3 * 2, rot, lds, w - 8, lane); return; }
  if (w < 4) {
    unsigned alo, ahi, blo, bhi;
    CODE_ADDR("_Z6k1_qkvPKtS0_S0_S0_Pc", alo, ahi);
    CODE_ADDR("_Z7k3_projPKcPKtS2_Pt", blo, bhi);
    const unsigned span = (bhi == ahi && blo > alo) ? blo - alo + 3584u : 0u;
    const unsigned off = min((unsigned)(w * 64 + lane) * 128u, span);
    const unsigned pf = *(const unsigned*)((((uint64_t)ahi << 32) | alo) + off);
    asm volatile("" :: "v"(pf));
  }
  f32x4 acc[4][4];
  if (swap) gemm_core<256, 3, true>(lds, w, lane, acc);
  else gemm_core<256, 3, false>(lds, w, lane, acc);

  const int wm = w >> 1, wn = w & 1, l15 = lane & 15, kq = lane >> 4;
  const int c0 = (nblk - seg * 5) * 128 + wn * 64;
  const int row0 = mblk * 256 + wm * 64;
  char* st = lds + (w < 6 ? 2 * 49152 + w * 8192 : (w - 6) * 8192);
  if (!swap) {
    const float sc = (prob && seg == 0) ? QSCALE : 1.f;
#pragma unroll
    for (int nb = 0; nb < 4; ++nb)
#pragma unroll
      for (int mb = 0; mb < 4; ++mb) {
        const f32x4 v = acc[nb][mb] * sc;
        *LDS_PTR(u32x2, st + (nb * 4 + kq) * 512 + (mb * 16 + l15) * 8) = (u32x2){pack2(v[0], v[1]), pack2(v[2], v[3])};
      }
    WAIT_LGKM0();
    asm volatile("" ::: "memory");
    if (prob && seg == 1) {
      float* kn = (float*)(ws + WS_KN) + (row0 >> 6);
      u32x2 qv[16];
#pragma unroll
      for (int q = 0; q < 16; ++q) qv[q] = *LDS_PTR(const u32x2, st + q * 512 + lane * 8);
      float s = 0.f;
      int hcur = c0 / 20;
#pragma unroll
      for (int q = 0; q < 16; ++q) {
        const unsigned u0 = qv[q][0], u1 = qv[q][1];
        float x;
        x = bf_lo(u0); s = fmaf(x, x, s); x = bf_hi(u0); s = fmaf(x, x, s);
        x = bf_lo(u1); s = fmaf(x, x, s); x = bf_hi(u1); s = fmaf(x, x, s);
        const bool hend = c0 + q * 4 + 4 == (hcur + 1) * 20;
        if (hend || q == 15) {
          s = rowmax16(s);
          s = fmaxf(fmaxf(lanef(s, 15), lanef(s, 31)), fmaxf(lanef(s, 47), lanef(s, 63)));
          const bool first = hcur * 20 >= c0;
          if (lane == 0) {
            kn[(hcur * 2 + (first ? 0 : 1)) * 32] = s;
            if (first && hend) kn[(hcur * 2 + 1) * 32] = 0.f;
          }
          s = 0.f; ++hcur;
        }
      }
    }
    const int tp = lane & 31;
#pragma unroll
    for (int it = 0; it < 8; ++it) {
      const u32x4 v = *LDS_PTR(const u32x4, st + it * 1024 + lane * 16);
      const int q = (c0 >> 2) + it * 2 + (lane >> 5), hh = q / 5, dq = q - hh * 5;
      if (!prob) {
        ST16(ws + WS_AQ + ((size_t)(hh * 15 + seg * 5 + dq) * S + row0 + 2 * tp) * 8, v);
      } else {
        char* g = ws + (seg ? WS_KV : WS_QB) + ((size_t)hh * 64 + (row0 >> 5) + (tp >> 4)) * (seg ? 3072 : 1536) + dq * 256 + (tp & 15) * 16;
        ST16(g, v);
        if (seg && dq == 4) ST16(g + 256, ((u32x4){0x3F80u, 0u, 0x3F80u, 0u}));
      }
    }
  } else {
#pragma unroll
    for (int nb = 0; nb < 4; ++nb)
#pragma unroll
      for (int mb = 0; mb < 4; ++mb) {
        const f32x4 v = acc[nb][mb];
        *LDS_PTR(u32x2, st + (mb * 2 + (kq & 1)) * 1024 + (nb * 16 + l15) * 16 + (kq >> 1) * 8) = (u32x2){pack2(v[0], v[1]), pack2(v[2], v[3])};
      }
    WAIT_LGKM0();
    asm volatile("" ::: "memory");
    const int c = c0 + lane, hh = c / 20, d = c - hh * 20;
    char* g = ws + WS_KV + ((size_t)hh * 64 + (row0 >> 5)) * 3072 + 1536 + d * 16;
#pragma unroll
    for (int it = 0; it < 8; ++it) {
      const u32x4 v = *LDS_PTR(const u32x4, st + it * 1024 + lane * 16);
      ST16(g + (it >> 2) * 3072 + (it & 3) * 384, v);
    }
#pragma unroll
    for (int it = 0; it < 2; ++it) {
      const int h2 = (c0 + 19) / 20 + it * 2 + (lane >> 5), blk = (lane >> 4) & 1, sh = (lane >> 2) & 3, d2 = 20 + (lane & 3);
      const unsigned o = d2 == 20 ? 0x3F803F80u : 0u;
      if (h2 * 20 < c0 + 64)
        ST16(ws + WS_KV + ((size_t)h2 * 64 + (row0 >> 5) + blk) * 3072 + 1536 + sh * 384 + d2 * 16, ((u32x4){o, o, o, o}));
    }
  }

}

constexpr int K2_HALFB = 8 * 3072;
constexpr int K2_STAGE = 2 * K2_HALFB;
constexpr int K2_LOC = 15 * 640;
constexpr int K2_LDS = 3 * K2_STAGE;
#define MFMA32(a, b, c) __builtin_amdgcn_mfma_f32_32x32x16_bf16(a, b, c, 0, 0, 0)

DI bf16x8 cat44(bf16x4 a, bf16x4 b) { return __builtin_shufflevector(a, b, 0, 1, 2, 3, 4, 5, 6, 7); }
DI void exp_pack(const f32x16& s, bf16x8& p0, bf16x8& p1) {
  u32x4 a, b;
#pragma unroll
  for (int i = 0; i < 4; ++i) {
    a[i] = pack2(__builtin_amdgcn_exp2f(s[2 * i]), __builtin_amdgcn_exp2f(s[2 * i + 1]));
    b[i] = pack2(__builtin_amdgcn_exp2f(s[8 + 2 * i]), __builtin_amdgcn_exp2f(s[8 + 2 * i + 1]));
  }
  p0 = __builtin_bit_cast(bf16x8, a);
  p1 = __builtin_bit_cast(bf16x8, b);
}
DI float swap32(float x) {
  const auto r = __builtin_amdgcn_permlane32_swap(__float_as_uint(x), __float_as_uint(x), false, false);
  return __uint_as_float((threadIdx.x & 32) ? r[0] : r[1]);
}
DI float sumsq8(bf16x8 f) {
  const u32x4 u = __builtin_bit_cast(u32x4, f);
  float s = 0.f;
#pragma unroll
  for (int i = 0; i < 4; ++i) { float x = bf_lo(u[i]); s = fmaf(x, x, s); x = bf_hi(u[i]); s = fmaf(x, x, s); }
  return s;
}

DI void attn_fallback(const char* kvh, bf16x8 q0, bf16x8 q1, int lane, f32x16& O) {
  const int r32 = lane & 31, h = lane >> 5;
  const int dv = r32 < 24 ? r32 : (r32 < 28 ? r32 - 4 : r32 - 12);
  float m = -INFINITY;
  O = (f32x16){};
#pragma unroll 1
  for (int t = 0; t < 64; ++t) {
    const char* g = kvh + (size_t)t * 3072;
    const bf16x8 k0 = cat44(*(const bf16x4*)(g + h * 512 + r32 * 8), *(const bf16x4*)(g + h * 512 + 256 + r32 * 8));
    const bf16x8 k1 = cat44(*(const bf16x4*)(g + 1024 + r32 * 8), *(const bf16x4*)(g + 1280 + r32 * 8));
    const bf16x8 v0 = *(const bf16x8*)(g + 1536 + h * 384 + dv * 16);
    const bf16x8 v1 = *(const bf16x8*)(g + 1536 + 768 + h * 384 + dv * 16);
    f32x16 s = MFMA32(k0, q0, (f32x16){});
    s = MFMA32(k1, q1, s);
    float pm = s[0];
#pragma unroll
    for (int i = 1; i < 16; ++i) pm = fmaxf(pm, s[i]);
    pm = fmaxf(pm, swap32(pm));
    const float mn = fmaxf(m, pm);
    const float alpha = __builtin_amdgcn_exp2f(m - mn);
    m = mn;
#pragma unroll
    for (int i = 0; i < 16; ++i) { s[i] -= mn; O[i] *= alpha; }
    bf16x8 p0, p1;
    exp_pack(s, p0, p1);
    O = MFMA32(v0, p0, O);
    O = MFMA32(v1, p1, O);
  }
}

template <bool MASK>
DI void local_attn(const char* me, int s, float (&o)[20]) {
  u32x2 q[5], kv[8][5];
#pragma unroll
  for (int i = 0; i < 5; ++i) q[i] = *LDS_PTR(const u32x2, me + i * 640);
#pragma unroll
  for (int j = 0; j < 8; ++j) {
    const char* kr = me - (MASK ? min(2 * j, s) : 2 * j) * 8 + 5 * 640;
#pragma unroll
    for (int i = 0; i < 5; ++i) kv[j][i] = *LDS_PTR(const u32x2, kr + i * 640);
  }
  SBAR();
  float lg[8];
  float mx = -INFINITY;
#pragma unroll
  for (int j = 0; j < 8; ++j) {
    float a0 = 0.f, a1 = 0.f;
#pragma unroll
    for (int i = 0; i < 5; ++i) {
      const unsigned x0 = kv[j][i][0], x1 = kv[j][i][1], y0 = q[i][0], y1 = q[i][1];
      a0 = dot2(x0, y0, a0);
      a1 = dot2(x1, y1, a1);
    }
    lg[j] = (a0 + a1) * QSCALE;
    if (MASK && s < 2 * j) lg[j] = -INFINITY;
    mx = fmaxf(mx, lg[j]);
  }
  SBAR();
#pragma unroll
  for (int j = 0; j < 8; ++j) {
    const char* vr = me - (MASK ? min(2 * j, s) : 2 * j) * 8 + 10 * 640;
#pragma unroll
    for (int i = 0; i < 5; ++i) kv[j][i] = *LDS_PTR(const u32x2, vr + i * 640);
  }
  SBAR();
  float sum = 0.f;
#pragma unroll
  for (int j = 0; j < 8; ++j) { lg[j] = __builtin_amdgcn_exp2f(lg[j] - mx); sum += lg[j]; }
  const float inv = 1.f / sum;
#pragma unroll
  for (int i = 0; i < 20; ++i) o[i] = 0.f;
#pragma unroll
  for (int j = 0; j < 8; ++j) {
    const unsigned pl = pack2(lg[j] * inv, 0.f), ph = pl << 16;
#pragma unroll
    for (int i = 0; i < 5; ++i) {
      const unsigned x0 = kv[j][i][0], x1 = kv[j][i][1];
      o[4 * i + 0] = dot2(x0, pl, o[4 * i + 0]);
      o[4 * i + 1] = dot2(x0, ph, o[4 * i + 1]);
      o[4 * i + 2] = dot2(x1, pl, o[4 * i + 2]);
      o[4 * i + 3] = dot2(x1, ph, o[4 * i + 3]);
    }
  }
}

__global__ __launch_bounds__(768, 3) void k2_attn(char* __restrict__ ws, const char* __restrict__ woA, const char* __restrict__ woB) {
  extern __shared__ __attribute__((aligned(16))) char lds[];
  const int tid = threadIdx.x, lane = tid & 63;
  const int w = __builtin_amdgcn_readfirstlane(tid >> 6);


  if (w >= 8) {
    __builtin_amdgcn_s_setprio(3);
    const int task = blockIdx.x * 4 + (w - 8), hA = task >> 5, r0 = (task & 31) * 64;
    char* L = lds + 2 * K2_STAGE + (w - 8) * K2_LOC;
    const char* g = ws + WS_AQ + ((ptrdiff_t)hA * 15 * S + r0 - 16) * 8;
    __builtin_amdgcn_s_barrier();
    if (lane < 40) {
#pragma unroll
      for (int i = 0; i < 15; ++i) glds16(g + (size_t)i * S * 8, lane * 16, (unsigned)(uintptr_t)L + i * 640);
    }
    WAIT_VM(0);
    const unsigned line = min(((blockIdx.x >> 3) * 4 + (w - 8)) * 64 + lane, 6399u);
    const unsigned pf = *(const unsigned*)(((blockIdx.x & 4) ? woB : woA) + (size_t)line * 128);
    SBAR();
    const int s = r0 + lane;
    float o[20];
    if (r0 == 0) local_attn<true>(L + (lane + 16) * 8, s, o); else local_attn<false>(L + (lane + 16) * 8, s, o);
    char* og = ws + WS_O + (size_t)s * (D * 2) + hA * 40;
#pragma unroll
    for (int i = 0; i < 5; ++i) *(u32x2*)(og + i * 8) = (u32x2){pack2(o[4 * i], o[4 * i + 1]), pack2(o[4 * i + 2], o[4 * i + 3])};
    asm volatile("" :: "v"(pf));

    return;
  }

  const int xcd = blockIdx.x & 7, slot = blockIdx.x >> 3;
  const int head = xcd * 4 + (slot >> 3), qc = slot & 7;
  const int qg = w & 3, kh = w >> 2;
  const int r32 = lane & 31, h = lane >> 5;
  const char* kvh = ws + WS_KV + (size_t)head * 64 * 3072;

  const int dhalf = w >> 2, doff = (w & 3) * 6144;
  const char* dsrc = kvh + (size_t)dhalf * 32 * 3072 + doff;
  const unsigned ddst = (unsigned)(uintptr_t)lds + dhalf * K2_HALFB + doff;
  auto stage = [&](int i) {
    const unsigned bo = (i == 3 ? 0 : i) * K2_STAGE;
#pragma unroll
    for (int j = 0; j < 6; ++j) glds16(dsrc + (size_t)i * K2_HALFB + j * 1024, lane * 16, ddst + bo + j * 1024);
  };
  stage(0);
  stage(1);

  bf16x8 qa0, qa1, qb0, qb1;
  {
    const char* qg_ = ws + WS_QB + ((size_t)head * 64 + qc * 8 + qg * 2) * 1536;
    const bf16x4 z4 = {};
    qa0 = cat44(*(const bf16x4*)(qg_ + h * 512 + r32 * 8), *(const bf16x4*)(qg_ + h * 512 + 256 + r32 * 8));
    qa1 = cat44(*(const bf16x4*)(qg_ + 1024 + r32 * 8), z4);
    qb0 = cat44(*(const bf16x4*)(qg_ + 1536 + h * 512 + r32 * 8), *(const bf16x4*)(qg_ + 1536 + h * 512 + 256 + r32 * 8));
    qb1 = cat44(*(const bf16x4*)(qg_ + 1536 + 1024 + r32 * 8), z4);
    float kn = rowmax16(((const float*)(ws + WS_KN))[head * 64 + lane]);
    kn = fmaxf(lanef(kn, 15), lanef(kn, 31)) + fmaxf(lanef(kn, 47), lanef(kn, 63));
    if (h) { qa1 = (bf16x8){}; qb1 = (bf16x8){}; }
    float na = sumsq8(qa0) + sumsq8(qa1), nb = sumsq8(qb0) + sumsq8(qb1);
    na += swap32(na);
    nb += swap32(nb);
    const float ra = sqrtf(na * kn) * 1.01f - 31.f, rb = sqrtf(nb * kn) * 1.01f - 31.f;
    if (!h) {
      qa1[4] = (short)(pack2(-ra, 0.f) & 0xffff);
      qb1[4] = (short)(pack2(-rb, 0.f) & 0xffff);
    }
  }
  const int dv = min(r32, 21);
  const int koff0 = kh * K2_HALFB + h * 512 + r32 * 8, koff1 = kh * K2_HALFB + 1024 + r32 * 8;
#define LDK(p) cat44(*LDS_PTR(const bf16x4, (p)), *LDS_PTR(const bf16x4, (p) + 256))
  const int voff0 = kh * K2_HALFB + 1536 + h * 384 + dv * 16, voff1 = voff0 + 768;

  f32x16 Oa = {}, Ob = {}, Sa, Sb;
  const f32x16 zero = {};
  bf16x8 kf[2][2], vf[2][2];
  WAIT_VM(6);
  RAW_BAR();

  __builtin_amdgcn_s_setprio(3);
  kf[0][0] = LDK(lds + koff0);
  kf[0][1] = LDK(lds + koff1);
  vf[0][0] = *LDS_PTR(const bf16x8, lds + voff0);
  vf[0][1] = *LDS_PTR(const bf16x8, lds + voff1);
  Sa = MFMA32(kf[0][0], qa0, zero);
  Sa = MFMA32(kf[0][1], qa1, Sa);
  u32x4 pk = {};
  u32x4 pc = {};
  float e12 = 0.f, e13 = 0.f, e14 = 0.f, e15 = 0.f;
  bf16x8 vlast = {};
#define EXP2(x) __builtin_amdgcn_exp2f(x)
#define BC8(x) __builtin_bit_cast(bf16x8, x)
#define E_(d, x) d = EXP2(x); PIN(d); SBAR();
#define C_(v, i, x, y) v[i] = pack2(x, y); PIN(v); SBAR();
#define HALF(SC, OC, OP, VP, SN, KN0, KN1, QN0, QN1, V0)                                         \
  do {                                                                                           \
    u32x4 p0;                                                                                    \
    float a0, a1, a2, a3, a4, a5, a6, a7, a8, a9, a10, a11;                                      \
    SBAR();                                                                                      \
    SN = MFMA32(KN0, QN0, zero); PIN(SN); SBAR();                                                \
    E_(a0, SC[0]) C_(pc, 2, e12, e13) E_(a1, SC[1]) C_(pc, 3, e14, e15) E_(a2, SC[2]) E_(a3, SC[3]) \
    OP = MFMA32(VP, BC8(pc), OP); PIN(OP); KEEP(pk); SBAR();                                     \
    E_(a4, SC[4]) E_(a5, SC[5]) C_(p0, 0, a0, a1) E_(a6, SC[6]) E_(a7, SC[7]) C_(p0, 1, a2, a3)  \
    SN = MFMA32(KN1, QN1, SN); PIN(SN); KEEP(pc); SBAR();                                        \
    E_(a8, SC[8]) C_(p0, 2, a4, a5) E_(a9, SC[9]) C_(p0, 3, a6, a7) E_(a10, SC[10]) E_(a11, SC[11]) \
    OC = MFMA32(V0, BC8(p0), OC); PIN(OC); SBAR();                                               \
    E_(e12, SC[12]) C_(pc, 0, a8, a9) E_(e13, SC[13]) C_(pc, 1, a10, a11) E_(e14, SC[14]) E_(e15, SC[15]) \
    pk = p0;                                                                                     \
  } while (0)
#pragma unroll 1
  for (int i = 0; i < 8; ++i) {
    const int sg = i >> 1;
    const char* b = lds + (sg == 3 ? 0 : sg) * K2_STAGE + (i & 1) * (4 * 3072);
    const char* bn = (i & 1) ? lds + (sg >= 2 ? 0 : sg + 1) * K2_STAGE : b + 4 * 3072;
#pragma unroll
    for (int bb = 0; bb < 4; ++bb) {
      const int c = bb & 1, n = c ^ 1;
      if (bb == 3 && (i & 1)) {
        WAIT_VM(0);
        RAW_BAR();
        if (sg + 2 < 4) stage(sg + 2);
      }
      if (bb == 3) { if (i & 1) __builtin_amdgcn_s_setprio(3); else __builtin_amdgcn_s_setprio(1); }
      else if (bb == 1) { if (i & 1) __builtin_amdgcn_s_setprio(0); else __builtin_amdgcn_s_setprio(2); }
      const char* nx = bb == 3 ? bn : b + (bb + 1) * 3072;
      kf[n][0] = LDK(nx + koff0);
      kf[n][1] = LDK(nx + koff1);
      HALF(Sa, Oa, Ob, vlast, Sb, kf[c][0], kf[c][1], qb0, qb1, vf[c][0]);
      vf[n][0] = *LDS_PTR(const bf16x8, nx + voff0);
      vf[n][1] = *LDS_PTR(const bf16x8, nx + voff1);
      HALF(Sb, Ob, Oa, vf[c][1], Sa, kf[n][0], kf[n][1], qa0, qa1, vf[c][0]);
      vlast = vf[c][1];
    }
  }
  pc[2] = pack2(e12, e13);
  pc[3] = pack2(e14, e15);
  Ob = MFMA32(vlast, BC8(pc), Ob);

  RAW_BAR();
  {
    float* ex = (float*)lds + (size_t)((qg * 2 + kh) * 16) * 64 + lane;
#pragma unroll
    for (int r = 0; r < 16; ++r) *LDS_PTR(float, ex + r * 64) = kh ? Oa[r] : Ob[r];
  }
  __syncthreads();
  f32x16 O;
  {
    const float* ex = (const float*)lds + (size_t)((qg * 2 + (kh ^ 1)) * 16) * 64 + lane;
#pragma unroll
    for (int r = 0; r < 16; ++r) O[r] = (kh ? Ob[r] : Oa[r]) + *LDS_PTR(const float, ex + r * 64);
  }
  float l = h ? O[8] : 0.f;
  l += swap32(l);
  float amax = fabsf(l);
#pragma unroll
  for (int r = 0; r < 12; ++r) amax = fmaxf(amax, fabsf(O[r]));
  const bool bad = !(l >= 8.4703294725430034e-22f  ) || !(amax <= 3.0e38f);
  if (__any(bad)) {
    bf16x8 f0 = kh ? qb0 : qa0, f1 = kh ? qb1 : qa1;
    if (!h) f1[4] = 0;
    attn_fallback(kvh, f0, f1, lane, O);
    l = h ? O[8] : 0.f;
    l += swap32(l);
  }
  const float inv = 1.f / l;
  const int q = (qc * 8 + qg * 2 + kh) * 32 + r32;
  char* og = ws + WS_O + ((size_t)S + q) * (D * 2) + head * 40 + h * 8;
#pragma unroll
  for (int g = 0; g < 3; ++g) {
    if (g < 2 || !h)
      *(u32x2*)(og + g * 16) = (u32x2){pack2(O[4 * g] * inv, O[4 * g + 1] * inv), pack2(O[4 * g + 2] * inv, O[4 * g + 3] * inv)};
  }

}

constexpr int K3_LDS = 5 * (128 * 128 + 64 * 256);

__global__ __launch_bounds__(768, 3) void k3_proj(const char* __restrict__ ws, const unsigned short* __restrict__ woA,
                                                  const unsigned short* __restrict__ woB, unsigned short* __restrict__ out) {
  extern __shared__ __attribute__((aligned(16))) char lds[];
  const int tid = threadIdx.x, lane = tid & 63;
  const int w = __builtin_amdgcn_readfirstlane(tid >> 6);
  const int xcd = blockIdx.x & 7, slot = blockIdx.x >> 3;
  const int mloc = slot / 5, nblk = slot - mloc * 5;
  const int prob = xcd >> 2, mblk = (xcd & 3) * 4 + mloc;
  const int rot = (2 * nblk + 3 * mloc) % 10;
  const char* X = ws + WS_O + ((size_t)prob * S + mblk * 128) * (D * 2);
  const char* W = (const char*)(prob ? woB : woA) + nblk * 256;

  if (w >= 8) { gemm_loader<128, 5>(X, W, D * 2, rot, lds, w - 8, lane); return; }
  f32x4 acc[4][2];
  gemm_core<128, 5, false>(lds, w, lane, acc);
  const int wm = w >> 1, wn = w & 1, l15 = lane & 15, kq = lane >> 4;
  char* st = lds + 2 * 32768 + w * 4096;
#pragma unroll
  for (int nb = 0; nb < 4; ++nb)
#pragma unroll
    for (int mb = 0; mb < 2; ++mb) {
      const f32x4 v = acc[nb][mb];
      const int r = mb * 16 + l15;
      *LDS_PTR(u32x2, st + r * 128 + (((nb * 2 + (kq >> 1)) ^ (r & 7)) << 4) + (kq & 1) * 8) = (u32x2){pack2(v[0], v[1]), pack2(v[2], v[3])};
    }
  WAIT_LGKM0();
  asm volatile("" ::: "memory");
#pragma unroll
  for (int it = 0; it < 4; ++it) {
    const int r = it * 8 + (lane >> 3), c = lane & 7;
    const u32x4 v = *LDS_PTR(const u32x4, st + r * 128 + ((c ^ (r & 7)) << 4));
    ST16(out + (size_t)(mblk * 128 + wm * 32 + r) * (2 * D) + prob * D + nblk * 128 + wn * 64 + c * 8, v);
  }

}

extern "C" void kernel_launch(void* const* d_in, const int* in_sizes, int n_in,
                              void* d_out, int out_size, void* d_ws, size_t ws_size,
                              hipStream_t stream) {
  (void)in_sizes; (void)n_in; (void)out_size; (void)ws_size;
  const unsigned short* xA = (const unsigned short*)d_in[0];
  const unsigned short* wA = (const unsigned short*)d_in[1];
  const unsigned short* oA = (const unsigned short*)d_in[2];
  const unsigned short* xB = (const unsigned short*)d_in[3];
  const unsigned short* wB = (const unsigned short*)d_in[4];
  const unsigned short* oB = (const unsigned short*)d_in[5];
  char* ws = (char*)d_ws;
  (void)hipFuncSetAttribute((const void*)k1_qkv, hipFuncAttributeMaxDynamicSharedMemorySize, K1_LDS);
  (void)hipFuncSetAttribute((const void*)k2_attn, hipFuncAttributeMaxDynamicSharedMemorySize, K2_LDS);
  (void)hipFuncSetAttribute((const void*)k3_proj, hipFuncAttributeMaxDynamicSharedMemorySize, K3_LDS);
  hipLaunchKernelGGL(k1_qkv, dim3(240), dim3(768), K1_LDS, stream, xA, wA, xB, wB, ws);
  hipLaunchKernelGGL(k2_attn, dim3(256), dim3(768), K2_LDS, stream, ws, (const char*)oA, (const char*)oB);
  hipLaunchKernelGGL(k3_proj, dim3(160), dim3(768), K3_LDS, stream, (const char*)ws, oA, oB, (unsigned short*)d_out);
}
```
